```python
import torch

def _softmax(x, axis=-1):
    return torch.softmax(x, dim=axis)
def _gelu(x):
    return torch.nn.functional.gelu(x, approximate="tanh")
def _silu(x):
    return torch.nn.functional.silu(x)
def _sigmoid(x):
    return torch.sigmoid(x)
def _layernorm(x, axis=-1, eps=1e-5):
    m = x.mean(dim=axis, keepdim=True)
    v = x.var(dim=axis, keepdim=True, unbiased=False)
    return (x - m) / torch.sqrt(v + eps)
def _scatter_add(tgt, idx, src, axis=0):
    return tgt.clone().index_add_(axis, idx, src)
def _segment_sum(src, seg, n):
    out = torch.zeros((n,) + src.shape[1:], dtype=src.dtype)
    return out.index_add_(0, seg, src)
def _pad(x, pw):
    flat = tuple(v for pair in reversed(pw) for v in pair)
    return torch.nn.functional.pad(x, flat)

kernel_name = 'standalone__window_attn_swin__vision'

def setup_inputs():

    _w = (lambda a: (lambda t: t.to(torch.bfloat16) if t.is_floating_point() else t)(torch.as_tensor(a)))
    H = W = 512
    C = 96
    ws = 8
    nh = 4
    shift = ws // 2
    coords = torch.stack(torch.meshgrid(torch.arange(ws), torch.arange(ws), indexing='ij'), 0).reshape(2, -1)
    rel = coords[:, :, None] - coords[:, None, :]
    rel[0] += ws - 1
    rel[1] += ws - 1
    rel[0] *= 2 * ws - 1
    rp_idx = rel.sum(0).to(torch.int64)
    img_mask = torch.zeros((1, H, W, 1), dtype=float)
    cnt = 0
    for hs in (slice(0, H - ws), slice(H - ws, H - shift), slice(H - shift, H)):
        for vs in (slice(0, W - ws), slice(W - ws, W - shift), slice(W - shift, W)):
            img_mask[:, hs, vs, :] = cnt
            cnt += 1
    mw = img_mask.reshape(1, H // ws, ws, W // ws, ws, 1).permute(0, 1, 3, 2, 4, 5).reshape(-1, ws * ws)
    am = mw[:, :, None] - mw[:, None, :]
    attn_mask = torch.where(am != 0, -100.0, 0.0).to(float)
    return {
        "x": _w((lambda *s: torch.randn(*s, dtype=torch.bfloat16) * 1.6)(1, H, W, C)),
        "wqkv": _w((lambda *s: torch.randn(*s, dtype=torch.bfloat16) * 1.6)(C, 3 * C) * 0.02),
        "wo": _w((lambda *s: torch.randn(*s, dtype=torch.bfloat16) * 0.05)(C, C) * 0.02),
        "rpb": _w((lambda *s: torch.randn(*s, dtype=torch.bfloat16) * 0.4)((2 * ws - 1) * (2 * ws - 1), nh)),
        "rp_idx": _w(rp_idx),
        "attn_mask": _w(attn_mask),
    }

def reference(x, wqkv, wo, rpb, rp_idx, attn_mask):
    B, H, W, C = x.shape
    N = rp_idx.shape[0]
    ws = int(round(N ** 0.5))
    nh = rpb.shape[-1]
    hd = C // nh
    shift = ws // 2
    xs = torch.roll(x, (-shift, -shift), (1, 2))
    nWh = H // ws
    nWw = W // ws
    xw = (lambda x, *d: x.permute(*d))(xs.reshape(B, nWh, ws, nWw, ws, C), 0, 1, 3, 2, 4, 5).reshape(B * nWh * nWw, N, C)
    qkv = (lambda x, *d: x.permute(*d))(torch.einsum('wnc,cd->wnd', xw, wqkv).reshape(-1, N, 3, nh, hd), 2, 0, 3, 1, 4)
    q, k, v = qkv[0], qkv[1], qkv[2]
    attn = torch.einsum('whid,whjd->whij', q, k) * (hd ** -0.5)
    bias = (lambda x, *d: x.permute(*d))(rpb[rp_idx.reshape(-1)].reshape(N, N, nh), 2, 0, 1)
    attn = attn + bias[None, :, :, :]
    attn = (attn.reshape(B, nWh * nWw, nh, N, N) + attn_mask[None, :, None, :, :]).reshape(-1, nh, N, N)
    attn = _softmax(attn, axis=-1)
    out = (lambda x, *d: x.permute(*d))(torch.einsum('whij,whjd->whid', attn, v), 0, 2, 1, 3).reshape(-1, N, C)
    out = torch.einsum('wnc,cd->wnd', out, wo)
    out = (lambda x, *d: x.permute(*d))(out.reshape(B, nWh, nWw, ws, ws, C), 0, 1, 3, 2, 4, 5).reshape(B, H, W, C)
    return torch.roll(out, (shift, shift), (1, 2))
```

```cpp
#include <hip/hip_runtime.h>
#include <hip/hip_bf16.h>
typedef __hip_bfloat16 bf16;
__device__ __forceinline__ float b2f(unsigned short u){ return __uint_as_float(((unsigned)u)<<16); }
__device__ __forceinline__ unsigned short f2b(float f){ unsigned u=__float_as_uint(f); u += 0x7fffu + ((u>>16)&1u); return (unsigned short)(u>>16); }
__global__ __launch_bounds__(256) void naive_k(const unsigned short* x, const unsigned short* wqkv, const unsigned short* wo, const unsigned short* rpb, const int* idx, const unsigned short* mask, unsigned short* out){
  __shared__ float xs[64*96];
  __shared__ float qkv[64*288];
  __shared__ float s[64*65];
  int w = blockIdx.x, wh = w>>6, ww = w&63, t = threadIdx.x;
  for(int e=t;e<64*96;e+=256){ int n=e/96,c=e%96; int r=(wh*8+(n>>3)+4)&511, cc=(ww*8+(n&7)+4)&511; xs[e]=b2f(x[((size_t)r*512+cc)*96+c]); }
  __syncthreads();
  for(int e=t;e<64*288;e+=256){ int n=e/288,d=e%288; float a=0; for(int c=0;c<96;++c) a+=xs[n*96+c]*b2f(wqkv[c*288+d]); qkv[e]=b2f(f2b(a)); }
  __syncthreads();
  for(int h=0;h<4;++h){
    for(int e=t;e<4096;e+=256){ int i=e>>6,j=e&63; float a=0; for(int d=0;d<24;++d) a+=qkv[i*288+h*24+d]*qkv[j*288+96+h*24+d];
      a = a*0.20412414523193154f + b2f(rpb[idx[e]*4+h]) + b2f(mask[(size_t)w*4096+e]); s[i*65+j]=a; }
    __syncthreads();
    if(t<64){ float m=-INFINITY; for(int j=0;j<64;++j) m=fmaxf(m,s[t*65+j]); float l=0; for(int j=0;j<64;++j){ float p=__expf(s[t*65+j]-m); s[t*65+j]=p; l+=p;} float r=1.f/l; for(int j=0;j<64;++j) s[t*65+j]=b2f(f2b(s[t*65+j]*r)); }
    __syncthreads();
    for(int e=t;e<64*24;e+=256){ int i=e/24,d=e%24; float a=0; for(int j=0;j<64;++j) a+=s[i*65+j]*qkv[j*288+192+h*24+d]; xs[i*96+h*24+d]=b2f(f2b(a)); }
    __syncthreads();
  }
  for(int e=t;e<64*96;e+=256){ int n=e/96,c=e%96; float a=0; for(int d=0;d<96;++d) a+=xs[n*96+d]*b2f(wo[d*96+c]);
    int r=(wh*8+(n>>3)+4)&511, cc=(ww*8+(n&7)+4)&511; out[((size_t)r*512+cc)*96+c]=f2b(a); }
}
extern "C" void kernel_launch(void* const* d_in, const int* in_sizes, int n_in, void* d_out, int out_size, void* d_ws, size_t ws_size, hipStream_t stream) {
  naive_k<<<4096,256,0,stream>>>((const unsigned short*)d_in[0],(const unsigned short*)d_in[1],(const unsigned short*)d_in[2],(const unsigned short*)d_in[3],(const int*)d_in[4],(const unsigned short*)d_in[5],(unsigned short*)d_out);
}
```

```cpp
#define PRIO 3
#define ZPAD 1
#define CPF 1
#define AUX_X 0
#define AUX_M 0
#define AUX_S 2
#include <hip/hip_runtime.h>
#include <hip/hip_bf16.h>

typedef __attribute__((ext_vector_type(2))) float f32x2;
typedef __attribute__((ext_vector_type(4))) float f32x4;
typedef __attribute__((ext_vector_type(16))) float f32x16;
typedef __attribute__((ext_vector_type(2))) __bf16 bf2;
typedef __attribute__((ext_vector_type(8))) short s16x8;
typedef __attribute__((ext_vector_type(4))) short s16x4;
typedef __attribute__((ext_vector_type(2))) unsigned u32x2;
typedef __attribute__((ext_vector_type(3))) unsigned u32x3;
typedef __attribute__((ext_vector_type(4))) unsigned u32x4;
typedef __attribute__((ext_vector_type(4))) int i32x4;
#define AS1 __attribute__((address_space(1)))
#define AS3 __attribute__((address_space(3)))
#define DI __device__ __forceinline__
#if defined(__HIP_DEVICE_COMPILE__)
#define NOPK __attribute__((target("no-packed-fp32-ops")))
#else
#define NOPK
#endif

constexpr int ROWB = 512 * 192;
constexpr int OSTR = 208;
constexpr int L_X = 0, L_O = 24576, L_WO = L_O + 2 * 64 * OSTR, L_M = L_WO + 18432, L_TAB = L_M + 8192, L_FLAG = L_TAB + 3600, L_END = L_FLAG + 32;
constexpr int L_STG = 12288;
constexpr int NWG = 512, NIT = 8;
constexpr float LOG2E = 1.4426950408889634f;
constexpr float QSCALE = 0.20412414523193154f * LOG2E;

DI unsigned pk(float a, float b) { f32x2 v = {a, b}; return __builtin_bit_cast(unsigned, __builtin_convertvector(v, bf2)); }
DI s16x8 as8(u32x4 v) { return __builtin_bit_cast(s16x8, v); }
DI s16x4 as4(u32x2 v) { return __builtin_bit_cast(s16x4, v); }
#define MFMA32(a, b, c) __builtin_amdgcn_mfma_f32_32x32x16_bf16((a), (b), (c), 0, 0, 0)
#define MFMA32K8(a, b, c) __builtin_amdgcn_mfma_f32_32x32x8bf16_1k((a), (b), (c), 0, 0, 0)
#define MFMA16(a, b, c) __builtin_amdgcn_mfma_f32_16x16x32_bf16((a), (b), (c), 0, 0, 0)
#define U2F(x) __builtin_bit_cast(float, (unsigned)(x))
#define F2U(x) __builtin_bit_cast(unsigned, (float)(x))
#define ANY(x) (__builtin_amdgcn_ballot_w64(x) != 0)
#define ST(k)
#define AC(k)
#define SBAR() __builtin_amdgcn_sched_barrier(0)

DI int lane_again() { int l; asm volatile("v_mbcnt_lo_u32_b32 %0, -1, 0\n\tv_mbcnt_hi_u32_b32 %0, -1, %0" : "=v"(l)); return l; }
DI f32x16 exact_tile(const u32x4 (&K0)[2], const u32x2 (&K1)[2], const u32x4 (&V)[2][2], u32x4 q0, u32x2 q1, const f32x16 (&bias)[2], __amdgpu_buffer_rsrc_t rm, int mv0, int ms) {
    f32x16 s[2];
    float m = -INFINITY;
#pragma unroll
    for (int kt = 0; kt < 2; ++kt) {
        s[kt] = MFMA32(as8(K0[kt]), as8(q0), bias[kt]);
        s[kt] = MFMA32K8(as4(K1[kt]), as4(q1), s[kt]);
#pragma unroll
        for (int g = 0; g < 4; ++g) {
            const u32x2 mv = __builtin_amdgcn_raw_buffer_load_b64(rm, mv0 + kt * 64 + g * 16, ms, 0);
            s[kt][4 * g + 0] = __builtin_fmaf(U2F(mv[0] << 16), LOG2E, s[kt][4 * g + 0]);
            s[kt][4 * g + 1] = __builtin_fmaf(U2F(mv[0] & 0xffff0000u), LOG2E, s[kt][4 * g + 1]);
            s[kt][4 * g + 2] = __builtin_fmaf(U2F(mv[1] << 16), LOG2E, s[kt][4 * g + 2]);
            s[kt][4 * g + 3] = __builtin_fmaf(U2F(mv[1] & 0xffff0000u), LOG2E, s[kt][4 * g + 3]);
        }
#pragma unroll
        for (int k = 0; k < 16; ++k) m = __builtin_fmaxf(m, s[kt][k]);
        SBAR();
    }
    const auto sw = __builtin_amdgcn_permlane32_swap(F2U(m), F2U(m), false, false);
    m = __builtin_fmaxf(U2F(sw[0]), U2F(sw[1]));
    f32x16 o = {};
#pragma unroll
    for (int kt = 0; kt < 2; ++kt)
#pragma unroll
        for (int s2 = 0; s2 < 2; ++s2) {
            u32x4 p;
#pragma unroll
            for (int k = 0; k < 4; ++k) p[k] = pk(__builtin_amdgcn_exp2f(s[kt][8 * s2 + 2 * k] - m), __builtin_amdgcn_exp2f(s[kt][8 * s2 + 2 * k + 1] - m));
            o = MFMA32(as8(V[kt][s2]), as8(p), o);
        }
    return o;
}

DI void put_o(AS3 char* ow, const f32x16& o) {
    const float ri = __builtin_amdgcn_rcpf(o[12]);
#pragma unroll
    for (int g = 0; g < 3; ++g) *(AS3 u32x2*)(ow + 8 * g) = (u32x2){pk(o[4 * g] * ri, o[4 * g + 1] * ri), pk(o[4 * g + 2] * ri, o[4 * g + 3] * ri)};
}

__global__ __launch_bounds__(256, 2) NOPK void swin_kernel(const char* __restrict__ x, const char* __restrict__ wqkv, const unsigned short* __restrict__ wo,
                                                          const u32x2* __restrict__ rpb, const i32x4* __restrict__ idx, const char* __restrict__ mask,
                                                          char* __restrict__ out
    ) {
    __shared__ __attribute__((aligned(16))) char lds_raw[L_END];
    AS3 char* const lds = (AS3 char*)lds_raw;
    const int tid = __builtin_amdgcn_workitem_id_x(), lane = tid & 63, r = lane & 31, h = lane >> 5;
    const int wid = __builtin_amdgcn_readfirstlane(tid >> 6);
    const int b = __builtin_amdgcn_workgroup_id_x();

    const __amdgpu_buffer_rsrc_t rx = __builtin_amdgcn_make_buffer_rsrc((void*)x, (short)0, 512 * ROWB, 0x00020000);
    const __amdgpu_buffer_rsrc_t rm = __builtin_amdgcn_make_buffer_rsrc((void*)mask, (short)0, 4096 * 8192, 0x00020000);
    const __amdgpu_buffer_rsrc_t ro = __builtin_amdgcn_make_buffer_rsrc((void*)out, (short)0, 512 * ROWB, 0x00020000);
    auto win_h = [&](int i) __attribute__((always_inline)) { return (b >> 6) + 8 * i; };
    auto win_w = [&](int i) __attribute__((always_inline)) { return (b + 8 * i + (b >> 6)) & 63; };

    unsigned rel[3];
#pragma unroll
    for (int k = 0; k < 3; ++k) {
        const int p = 64 * k + lane, nl = p / 12, c = p - 12 * nl;
        rel[k] = (nl >> 3) * ROWB + (nl & 7) * 192 + 16 * (c ^ ((nl >> 2) & 3));
    }
    const unsigned l16 = lane * 16;
    auto issue_x = [&](int i) __attribute__((always_inline)) {
        const int wh = win_h(i), ww = win_w(i);
        const int xb = ((8 * wh + 4 + 2 * wid) & 511) * ROWB + (8 * ww + 4) * 192;
        AS3 char* dst = lds + L_X + (i & 1) * 12288 + wid * 3072;
        if (__builtin_expect(ww == 63, 0)) {
            const int l2 = lane_again();
#pragma unroll
            for (int k = 0; k < 3; ++k) {
                const int nl = (64 * k + l2) / 12;
                const int o = xb + (int)rel[k] - ((nl & 4) ? ROWB : 0);
                __builtin_amdgcn_raw_ptr_buffer_load_lds(rx, (AS3 void*)(dst + k * 1024), 16, o, 0, 0, AUX_X);
            }
        } else {
#pragma unroll
            for (int k = 0; k < 3; ++k)
                __builtin_amdgcn_raw_ptr_buffer_load_lds(rx, (AS3 void*)(dst + k * 1024), 16, rel[k], xb, 0, AUX_X);
        }
    };
    auto issue_m = [&](int i) __attribute__((always_inline)) {
        const int mb = (win_h(i) * 64 + win_w(i)) * 8192 + wid * 2048;
#pragma unroll
        for (int k = 0; k < 2; ++k)
            __builtin_amdgcn_raw_ptr_buffer_load_lds(rm, (AS3 void*)(lds + L_M + wid * 2048 + k * 1024), 16, l16, mb + k * 1024, 0, AUX_M);
    };
    auto put_flag = [&](int i, u32x4 a, u32x4 c) __attribute__((always_inline)) {
        const unsigned o = (a[0] | a[1] | a[2]) | (a[3] | c[0] | c[1]) | (c[2] | c[3]);
        const bool nz = ANY(o != 0);
        if (lane == 0) *(AS3 unsigned*)(lds + L_FLAG + (i & 1) * 16 + wid * 4) = nz ? 1u : 0u;
    };

    unsigned cpf_;
    {
        unsigned long long pc_;
        asm volatile("s_getpc_b64 %0" : "=s"(pc_));
        const unsigned o_ = (tid < 216 ? tid : 0) * 64;
        asm volatile("s_nop 4\n\tglobal_load_dword %0, %1, %2" : "=&v"(cpf_) : "v"(o_), "s"(pc_) : "memory");
    }
    ST(0);
    issue_x(0);
    u32x4 m0a, m0b;
    {
        const char* mb = mask + (size_t)(win_h(0) * 64 + win_w(0)) * 8192 + wid * 2048;
        m0a = __builtin_nontemporal_load((const u32x4*)(mb + l16));
        m0b = __builtin_nontemporal_load((const u32x4*)(mb + 1024 + l16));
    }
#pragma unroll
    for (int u = 0; u < 14; ++u) {
        const int q = wid + 4 * u;
        if (q < 54) __builtin_amdgcn_global_load_lds((const AS1 void*)(wqkv + q * 1024 + l16), (AS3 void*)(lds + L_STG + q * 1024), 16, 0, 0);
    }
    i32x4 id4[4];
#pragma unroll
    for (int u = 0; u < 4; ++u) id4[u] = idx[256 * u + tid];
    u32x2 rp = {0u, 0u};
    if (tid < 225) rp = rpb[tid];
    s16x4 wop[9];
    unsigned woa[9];
#pragma unroll
    for (int u = 0; u < 9; ++u) {
        const int id = tid + 256 * u, kq = id / 96, c = id - 96 * kq;
        const int d0 = 24 * (kq / 6) + 8 * (kq % 3) + 4 * ((kq % 6) / 3);
        const unsigned short* p = wo + d0 * 96 + c;
        wop[u] = (s16x4){(short)p[0], (short)p[96], (short)p[192], (short)p[288]};
        const int cl = c / 6, t6 = c - 6 * cl;
        woa[u] = L_WO + (t6 * 3 + (kq >> 3)) * 1024 + (16 * ((kq >> 1) & 3) + cl) * 16 + 8 * (kq & 1);
    }
#pragma unroll
    for (int u = 0; u < 4; ++u) {
        const int i = 16 * u + (tid >> 4), jq = tid & 15;
        int e[4];
#pragma unroll
        for (int k = 0; k < 4; ++k) { const int v = id4[u][k]; e[k] = (v < 0 ? v + 225 : v) * 4; }
        const u32x2 w2 = {(unsigned)e[0] | ((unsigned)e[1] << 16), (unsigned)e[2] | ((unsigned)e[3] << 16)};
        const int qt = i >> 5, kt = jq >> 3, g = (jq >> 1) & 3, hh = jq & 1;
        *(AS3 u32x2*)(lds + L_M + (((qt * 2 + kt) * 2 + (g >> 1)) * 64 + 32 * hh + (i & 31)) * 16 + (g & 1) * 8) = w2;
    }
    if (tid < 225) {
        *(AS3 float*)(lds + L_TAB + 0 * 900 + tid * 4) = U2F(rp[0] << 16) * LOG2E;
        *(AS3 float*)(lds + L_TAB + 1 * 900 + tid * 4) = U2F(rp[0] & 0xffff0000u) * LOG2E;
        *(AS3 float*)(lds + L_TAB + 2 * 900 + tid * 4) = U2F(rp[1] << 16) * LOG2E;
        *(AS3 float*)(lds + L_TAB + 3 * 900 + tid * 4) = U2F(rp[1] & 0xffff0000u) * LOG2E;
    }
    put_flag(0, m0a, m0b);
    ST(1);
    asm volatile("s_waitcnt vmcnt(0) lgkmcnt(0)\n\ts_barrier" ::: "memory");
    ST(2);
    asm volatile("" :: "v"(cpf_));

    s16x8 W[3][6];
    {
        AS3 char* wb = lds + L_STG + (8 * h + ((lane & 15) >> 2)) * 576 + (24 * wid + 16 * ((lane >> 4) & 1) + 4 * (lane & 3)) * 2;
#pragma unroll
        for (int P = 0; P < 3; ++P)
#pragma unroll
            for (int s = 0; s < 6; ++s) {
                const s16x4 lo = __builtin_amdgcn_ds_read_tr16_b64_v4i16((AS3 s16x4*)(wb + s * 9216 + P * 192));
                const s16x4 hi = __builtin_amdgcn_ds_read_tr16_b64_v4i16((AS3 s16x4*)(wb + s * 9216 + 2304 + P * 192));
                W[P][s] = __builtin_shufflevector(lo, hi, 0, 1, 2, 3, 4, 5, 6, 7);
            }
#pragma unroll
        for (int P = (ZPAD ? 0 : 2); P < 3; ++P)
#pragma unroll
        for (int s = 0; s < 6; ++s) {
            u32x4 v = __builtin_bit_cast(u32x4, W[P][s]);
#pragma unroll
            for (int k = 0; k < 4; ++k) v[k] = r < 24 ? v[k] : 0u;
            W[P][s] = as8(v);
        }
    }
    u32x4 ix[8];
#pragma unroll
    for (int u = 0; u < 8; ++u) ix[u] = *(AS3 u32x4*)(lds + L_M + u * 1024 + l16);
    asm volatile("s_waitcnt lgkmcnt(0)\n\ts_barrier" ::: "memory");
    SBAR();
    ST(3);
    issue_x(1);
    issue_m(1);
#pragma unroll
    for (int u = 0; u < 9; ++u) *(AS3 s16x4*)(lds + woa[u]) = wop[u];
    f32x16 bias[2][2];
    {
        AS3 char* tb = lds + L_TAB + wid * 900;
#pragma unroll
        for (int u = 0; u < 8; ++u)
#pragma unroll
            for (int k = 0; k < 4; ++k) {
                bias[u >> 2][(u >> 1) & 1][8 * (u & 1) + 2 * k] = *(AS3 float*)(tb + (ix[u][k] & 0xffffu));
                bias[u >> 2][(u >> 1) & 1][8 * (u & 1) + 2 * k + 1] = *(AS3 float*)(tb + (ix[u][k] >> 16));
            }
    }
    const unsigned ones = ((r & 27) == 24) ? 0x3f803f80u : 0u;
    const int gsw = (r >> 2) & 3;
    const unsigned xa0 = L_X + r * 192 + 16 * (h ^ gsw), xa1 = L_X + r * 192 + 16 * ((h ^ gsw) ^ 2);
    const unsigned owa = L_O + r * OSTR + 48 * wid + 24 * h;
    const unsigned ora = L_O + (16 * wid + 4 * (lane & 3) + ((lane & 15) >> 2)) * OSTR + 16 * (lane >> 4);
    const unsigned l12 = lane * 12;

    auto proj = [&](int i) __attribute__((always_inline)) {
        const unsigned oa = ora + (i & 1) * (64 * OSTR);
        s16x8 a[3];
#pragma unroll
        for (int s = 0; s < 3; ++s) a[s] = *(AS3 s16x8*)(lds + oa + 64 * s);
        f32x4 d[6];
#pragma unroll
        for (int t = 0; t < 6; ++t) {
            d[t] = (f32x4){0.f, 0.f, 0.f, 0.f};
#pragma unroll
            for (int s = 0; s < 3; ++s) d[t] = MFMA16(a[s], *(AS3 s16x8*)(lds + L_WO + (t * 3 + s) * 1024 + l16), d[t]);
        }
        const int wh = win_h(i), ww = win_w(i);
        const int ob = ((8 * wh + 4 + 2 * wid) & 511) * ROWB;
#pragma unroll
        for (int p = 0; p < 4; ++p) {
            const int o = ob + (p >> 1) * ROWB + ((8 * ww + 4 + 4 * (p & 1)) & 511) * 192;
            const u32x3 v = {pk(d[0][p], d[1][p]), pk(d[2][p], d[3][p]), pk(d[4][p], d[5][p])};
            __builtin_amdgcn_raw_buffer_store_b96(v, ro, l12, o, AUX_S);
        }
    };

    ST(4);
    for (int n = 0; n < NIT; ++n) {
        AC(5);
        if (n > 0) proj(n - 1);
        AC(0);
        __builtin_amdgcn_s_setprio(0);
        SBAR();
        u32x4 Q0[2], K0[2], V[2][2];
        u32x2 Q1[2], K1[2];
        const unsigned xo = (n & 1) * 12288;
#pragma unroll
        for (int tt = 0; tt < 2; ++tt) {
            s16x8 X[6];
#pragma unroll
            for (int s = 0; s < 6; ++s) X[s] = *(AS3 s16x8*)(lds + ((s & 1) ? xa1 : xa0) + xo + tt * 6144 + (s >> 1) * 64);
            f32x16 a = {}, c = {}, v = {};
#pragma unroll
            for (int s = 0; s < 6; ++s) a = MFMA32(W[0][s], X[s], a);
#pragma unroll
            for (int s = 0; s < 6; ++s) c = MFMA32(W[1][s], X[s], c);
#pragma unroll
            for (int s = 0; s < 6; ++s) v = MFMA32(X[s], W[2][s], v);
#pragma unroll
            for (int k = 0; k < 4; ++k) Q0[tt][k] = pk(a[2 * k] * QSCALE, a[2 * k + 1] * QSCALE);
#pragma unroll
            for (int k = 0; k < 2; ++k) Q1[tt][k] = pk(a[8 + 2 * k] * QSCALE, a[9 + 2 * k] * QSCALE);
#pragma unroll
            for (int k = 0; k < 4; ++k) K0[tt][k] = pk(c[2 * k], c[2 * k + 1]);
#pragma unroll
            for (int k = 0; k < 2; ++k) K1[tt][k] = pk(c[8 + 2 * k], c[9 + 2 * k]);
#pragma unroll
            for (int k = 0; k < 8; ++k) V[tt][k >> 2][k & 3] = pk(v[2 * k], v[2 * k + 1]) | ones;
            SBAR();
        }
        AC(1);
        __builtin_amdgcn_s_setprio(1);
        const u32x4 fl = *(AS3 u32x4*)(lds + L_FLAG + (n & 1) * 16);
        const bool masked = __builtin_amdgcn_readfirstlane(fl[0] | fl[1] | fl[2] | fl[3]) != 0;
        AS3 char* const ow = lds + owa + (n & 1) * (64 * OSTR);
        bool slow = masked;
        if (!masked) {
            bool bad = false;
#pragma unroll
            for (int qt = 0; qt < 2; ++qt) {
                f32x16 o = {};
#pragma unroll
                for (int kt = 0; kt < 2; ++kt) {
                    f32x16 s = MFMA32(as8(K0[kt]), as8(Q0[qt]), bias[qt][kt]);
                    s = MFMA32K8(as4(K1[kt]), as4(Q1[qt]), s);
#pragma unroll
                    for (int s2 = 0; s2 < 2; ++s2) {
                        u32x4 p;
#pragma unroll
                        for (int k = 0; k < 4; ++k) p[k] = pk(__builtin_amdgcn_exp2f(s[8 * s2 + 2 * k]), __builtin_amdgcn_exp2f(s[8 * s2 + 2 * k + 1]));
                        o = MFMA32(as8(V[kt][s2]), as8(p), o);
                    }
                }
                bad |= !(o[12] >= 0x1p-20f && o[12] <= 0x1p20f);
                put_o(ow + qt * (32 * OSTR), o);
            }
            slow = ANY(bad);
        }
        if (__builtin_expect(slow, 0)) {
            const int ms = (win_h(n) * 64 + win_w(n)) * 8192;
            const int l2 = lane_again();
            const int mv0 = (l2 & 31) * 128 + (l2 >> 5) * 8;
            put_o(ow, exact_tile(K0, K1, V, Q0[0], Q1[0], bias[0], rm, mv0, ms));
            SBAR();
            put_o(ow + 32 * OSTR, exact_tile(K0, K1, V, Q0[1], Q1[1], bias[1], rm, mv0, ms + 4096));
        }
        AC(2);
        if (n + 1 < NIT) {
            if (n == 0) asm volatile("s_waitcnt vmcnt(0)" ::: "memory"); else asm volatile("s_waitcnt vmcnt(4)" ::: "memory");
            AC(3);
            const u32x4 ma = *(AS3 u32x4*)(lds + L_M + wid * 2048 + l16), mb2 = *(AS3 u32x4*)(lds + L_M + wid * 2048 + 1024 + l16);
            put_flag(n + 1, ma, mb2);
        }
        asm volatile("s_waitcnt lgkmcnt(0)\n\ts_barrier" ::: "memory");
        AC(4);
        if (n + 2 < NIT) { issue_x(n + 2); issue_m(n + 2); }
        ST(5 + n);
    }
    proj(NIT - 1);
    ST(13);
}

extern "C" void kernel_launch(void* const* d_in, const int* in_sizes, int n_in, void* d_out, int out_size, void* d_ws, size_t ws_size, hipStream_t stream) {
    swin_kernel<<<NWG, 256, 0, stream>>>((const char*)d_in[0], (const char*)d_in[1], (const unsigned short*)d_in[2], (const u32x2*)d_in[3],
                                         (const i32x4*)d_in[4], (const char*)d_in[5], (char*)d_out
    );
}
```

```cpp
#define PRIO 3
#define ZPAD 1
#define CPF 0
#define TLBW 0
#define AUX_X 0
#define AUX_M 0
#define AUX_S 17
#include <hip/hip_runtime.h>
#include <hip/hip_bf16.h>

typedef __attribute__((ext_vector_type(2))) float f32x2;
typedef __attribute__((ext_vector_type(4))) float f32x4;
typedef __attribute__((ext_vector_type(16))) float f32x16;
typedef __attribute__((ext_vector_type(2))) __bf16 bf2;
typedef __attribute__((ext_vector_type(8))) short s16x8;
typedef __attribute__((ext_vector_type(4))) short s16x4;
typedef __attribute__((ext_vector_type(2))) unsigned u32x2;
typedef __attribute__((ext_vector_type(3))) unsigned u32x3;
typedef __attribute__((ext_vector_type(4))) unsigned u32x4;
typedef __attribute__((ext_vector_type(4))) int i32x4;
#define AS1 __attribute__((address_space(1)))
#define AS3 __attribute__((address_space(3)))
#define DI __device__ __forceinline__
#if defined(__HIP_DEVICE_COMPILE__)
#define NOPK __attribute__((target("no-packed-fp32-ops")))
#else
#define NOPK
#endif

constexpr int ROWB = 512 * 192;
constexpr int OSTR = 208;
constexpr int L_X = 0, L_O = 24576, L_WO = L_O + 2 * 64 * OSTR, L_M = L_WO + 18432, L_TAB = L_M + 8192, L_FLAG = L_TAB + 3600, L_END = L_FLAG + 32;
constexpr int L_STG = 12288;
constexpr int NWG = 512, NIT = 8;
constexpr float LOG2E = 1.4426950408889634f;
constexpr float QSCALE = 0.20412414523193154f * LOG2E;

DI unsigned pk(float a, float b) { f32x2 v = {a, b}; return __builtin_bit_cast(unsigned, __builtin_convertvector(v, bf2)); }
DI s16x8 as8(u32x4 v) { return __builtin_bit_cast(s16x8, v); }
DI s16x4 as4(u32x2 v) { return __builtin_bit_cast(s16x4, v); }
#define MFMA32(a, b, c) __builtin_amdgcn_mfma_f32_32x32x16_bf16((a), (b), (c), 0, 0, 0)
#define MFMA32K8(a, b, c) __builtin_amdgcn_mfma_f32_32x32x8bf16_1k((a), (b), (c), 0, 0, 0)
#define MFMA16(a, b, c) __builtin_amdgcn_mfma_f32_16x16x32_bf16((a), (b), (c), 0, 0, 0)
#define U2F(x) __builtin_bit_cast(float, (unsigned)(x))
#define F2U(x) __builtin_bit_cast(unsigned, (float)(x))
#define ANY(x) (__builtin_amdgcn_ballot_w64(x) != 0)
#define ST(k)
#define AC(k)
#define SBAR() __builtin_amdgcn_sched_barrier(0)

DI int lane_again() { int l; asm volatile("v_mbcnt_lo_u32_b32 %0, -1, 0\n\tv_mbcnt_hi_u32_b32 %0, -1, %0" : "=v"(l)); return l; }
DI f32x16 exact_tile(const u32x4 (&K0)[2], const u32x2 (&K1)[2], const u32x4 (&V)[2][2], u32x4 q0, u32x2 q1, const f32x16 (&bias)[2], __amdgpu_buffer_rsrc_t rm, int mv0, int ms) {
    f32x16 s[2];
    float m = -INFINITY;
#pragma unroll
    for (int kt = 0; kt < 2; ++kt) {
        s[kt] = MFMA32(as8(K0[kt]), as8(q0), bias[kt]);
        s[kt] = MFMA32K8(as4(K1[kt]), as4(q1), s[kt]);
#pragma unroll
        for (int g = 0; g < 4; ++g) {
            const u32x2 mv = __builtin_amdgcn_raw_buffer_load_b64(rm, mv0 + kt * 64 + g * 16, ms, 0);
            s[kt][4 * g + 0] = __builtin_fmaf(U2F(mv[0] << 16), LOG2E, s[kt][4 * g + 0]);
            s[kt][4 * g + 1] = __builtin_fmaf(U2F(mv[0] & 0xffff0000u), LOG2E, s[kt][4 * g + 1]);
            s[kt][4 * g + 2] = __builtin_fmaf(U2F(mv[1] << 16), LOG2E, s[kt][4 * g + 2]);
            s[kt][4 * g + 3] = __builtin_fmaf(U2F(mv[1] & 0xffff0000u), LOG2E, s[kt][4 * g + 3]);
        }
#pragma unroll
        for (int k = 0; k < 16; ++k) m = __builtin_fmaxf(m, s[kt][k]);
        SBAR();
    }
    const auto sw = __builtin_amdgcn_permlane32_swap(F2U(m), F2U(m), false, false);
    m = __builtin_fmaxf(U2F(sw[0]), U2F(sw[1]));
    f32x16 o = {};
#pragma unroll
    for (int kt = 0; kt < 2; ++kt)
#pragma unroll
        for (int s2 = 0; s2 < 2; ++s2) {
            u32x4 p;
#pragma unroll
            for (int k = 0; k < 4; ++k) p[k] = pk(__builtin_amdgcn_exp2f(s[kt][8 * s2 + 2 * k] - m), __builtin_amdgcn_exp2f(s[kt][8 * s2 + 2 * k + 1] - m));
            o = MFMA32(as8(V[kt][s2]), as8(p), o);
        }
    return o;
}

DI void put_o(AS3 char* ow, const f32x16& o) {
    const float ri = __builtin_amdgcn_rcpf(o[12]);
#pragma unroll
    for (int g = 0; g < 3; ++g) *(AS3 u32x2*)(ow + 8 * g) = (u32x2){pk(o[4 * g] * ri, o[4 * g + 1] * ri), pk(o[4 * g + 2] * ri, o[4 * g + 3] * ri)};
}

__global__ __launch_bounds__(256, 2) NOPK void swin_kernel(const char* __restrict__ x, const char* __restrict__ wqkv, const unsigned short* __restrict__ wo,
                                                          const u32x2* __restrict__ rpb, const i32x4* __restrict__ idx, const char* __restrict__ mask,
                                                          char* __restrict__ out
    ) {
    __shared__ __attribute__((aligned(16))) char lds_raw[L_END];
    AS3 char* const lds = (AS3 char*)lds_raw;
    const int tid = __builtin_amdgcn_workitem_id_x(), lane = tid & 63, r = lane & 31, h = lane >> 5;
    const int wid = __builtin_amdgcn_readfirstlane(tid >> 6);
    const int b = __builtin_amdgcn_workgroup_id_x();

    const __amdgpu_buffer_rsrc_t rx = __builtin_amdgcn_make_buffer_rsrc((void*)x, (short)0, 512 * ROWB, 0x00020000);
    const __amdgpu_buffer_rsrc_t rm = __builtin_amdgcn_make_buffer_rsrc((void*)mask, (short)0, 4096 * 8192, 0x00020000);
    const __amdgpu_buffer_rsrc_t ro = __builtin_amdgcn_make_buffer_rsrc((void*)out, (short)0, 512 * ROWB, 0x00020000);
    auto win_h = [&](int i) __attribute__((always_inline)) { return (b >> 6) + 8 * i; };
    auto win_w = [&](int i) __attribute__((always_inline)) { return (b + 8 * i + (b >> 6)) & 63; };

    unsigned rel[3];
#pragma unroll
    for (int k = 0; k < 3; ++k) {
        const int p = 64 * k + lane, nl = p / 12, c = p - 12 * nl;
        rel[k] = (nl >> 3) * ROWB + (nl & 7) * 192 + 16 * (c ^ ((nl >> 2) & 3));
    }
    const unsigned l16 = lane * 16;
    auto issue_x = [&](int i) __attribute__((always_inline)) {
        const int wh = win_h(i), ww = win_w(i);
        const int xb = ((8 * wh + 4 + 2 * wid) & 511) * ROWB + (8 * ww + 4) * 192;
        AS3 char* dst = lds + L_X + (i & 1) * 12288 + wid * 3072;
        if (__builtin_expect(ww == 63, 0)) {
            const int l2 = lane_again();
#pragma unroll
            for (int k = 0; k < 3; ++k) {
                const int nl = (64 * k + l2) / 12;
                const int o = xb + (int)rel[k] - ((nl & 4) ? ROWB : 0);
                __builtin_amdgcn_raw_ptr_buffer_load_lds(rx, (AS3 void*)(dst + k * 1024), 16, o, 0, 0, AUX_X);
            }
        } else {
#pragma unroll
            for (int k = 0; k < 3; ++k)
                __builtin_amdgcn_raw_ptr_buffer_load_lds(rx, (AS3 void*)(dst + k * 1024), 16, rel[k], xb, 0, AUX_X);
        }
    };
    auto issue_m = [&](int i) __attribute__((always_inline)) {
        const int mb = (win_h(i) * 64 + win_w(i)) * 8192 + wid * 2048;
#pragma unroll
        for (int k = 0; k < 2; ++k)
            __builtin_amdgcn_raw_ptr_buffer_load_lds(rm, (AS3 void*)(lds + L_M + wid * 2048 + k * 1024), 16, l16, mb + k * 1024, 0, AUX_M);
    };
    auto put_flag = [&](int i, u32x4 a, u32x4 c) __attribute__((always_inline)) {
        const unsigned o = (a[0] | a[1] | a[2]) | (a[3] | c[0] | c[1]) | (c[2] | c[3]);
        const bool nz = ANY(o != 0);
        if (lane == 0) *(AS3 unsigned*)(lds + L_FLAG + (i & 1) * 16 + wid * 4) = nz ? 1u : 0u;
    };

    ST(0);
    issue_x(0);
    u32x4 m0a, m0b;
    {
        const char* mb = mask + (size_t)(win_h(0) * 64 + win_w(0)) * 8192 + wid * 2048;
        m0a = __builtin_nontemporal_load((const u32x4*)(mb + l16));
        m0b = __builtin_nontemporal_load((const u32x4*)(mb + 1024 + l16));
    }
#pragma unroll
    for (int u = 0; u < 14; ++u) {
        const int q = wid + 4 * u;
        if (q < 54) __builtin_amdgcn_global_load_lds((const AS1 void*)(wqkv + q * 1024 + l16), (AS3 void*)(lds + L_STG + q * 1024), 16, 0, 0);
    }
    i32x4 id4[4];
#pragma unroll
    for (int u = 0; u < 4; ++u) id4[u] = idx[256 * u + tid];
    u32x2 rp = {0u, 0u};
    if (tid < 225) rp = rpb[tid];
    s16x4 wop[9];
    unsigned woa[9];
#pragma unroll
    for (int u = 0; u < 9; ++u) {
        const int id = tid + 256 * u, kq = id / 96, c = id - 96 * kq;
        const int d0 = 24 * (kq / 6) + 8 * (kq % 3) + 4 * ((kq % 6) / 3);
        const unsigned short* p = wo + d0 * 96 + c;
        wop[u] = (s16x4){(short)p[0], (short)p[96], (short)p[192], (short)p[288]};
        const int cl = c / 6, t6 = c - 6 * cl;
        woa[u] = L_WO + (t6 * 3 + (kq >> 3)) * 1024 + (16 * ((kq >> 1) & 3) + cl) * 16 + 8 * (kq & 1);
    }
#pragma unroll
    for (int u = 0; u < 4; ++u) {
        const int i = 16 * u + (tid >> 4), jq = tid & 15;
        int e[4];
#pragma unroll
        for (int k = 0; k < 4; ++k) { const int v = id4[u][k]; e[k] = (v < 0 ? v + 225 : v) * 4; }
        const u32x2 w2 = {(unsigned)e[0] | ((unsigned)e[1] << 16), (unsigned)e[2] | ((unsigned)e[3] << 16)};
        const int qt = i >> 5, kt = jq >> 3, g = (jq >> 1) & 3, hh = jq & 1;
        *(AS3 u32x2*)(lds + L_M + (((qt * 2 + kt) * 2 + (g >> 1)) * 64 + 32 * hh + (i & 31)) * 16 + (g & 1) * 8) = w2;
    }
    if (tid < 225) {
        *(AS3 float*)(lds + L_TAB + 0 * 900 + tid * 4) = U2F(rp[0] << 16) * LOG2E;
        *(AS3 float*)(lds + L_TAB + 1 * 900 + tid * 4) = U2F(rp[0] & 0xffff0000u) * LOG2E;
        *(AS3 float*)(lds + L_TAB + 2 * 900 + tid * 4) = U2F(rp[1] << 16) * LOG2E;
        *(AS3 float*)(lds + L_TAB + 3 * 900 + tid * 4) = U2F(rp[1] & 0xffff0000u) * LOG2E;
    }
    put_flag(0, m0a, m0b);
    ST(1);
    asm volatile("s_waitcnt vmcnt(0) lgkmcnt(0)\n\ts_barrier" ::: "memory");
    ST(2);

    s16x8 W[3][6];
    {
        AS3 char* wb = lds + L_STG + (8 * h + ((lane & 15) >> 2)) * 576 + (24 * wid + 16 * ((lane >> 4) & 1) + 4 * (lane & 3)) * 2;
#pragma unroll
        for (int P = 0; P < 3; ++P)
#pragma unroll
            for (int s = 0; s < 6; ++s) {
                const s16x4 lo = __builtin_amdgcn_ds_read_tr16_b64_v4i16((AS3 s16x4*)(wb + s * 9216 + P * 192));
                const s16x4 hi = __builtin_amdgcn_ds_read_tr16_b64_v4i16((AS3 s16x4*)(wb + s * 9216 + 2304 + P * 192));
                W[P][s] = __builtin_shufflevector(lo, hi, 0, 1, 2, 3, 4, 5, 6, 7);
            }
#pragma unroll
        for (int P = (ZPAD ? 0 : 2); P < 3; ++P)
#pragma unroll
        for (int s = 0; s < 6; ++s) {
            u32x4 v = __builtin_bit_cast(u32x4, W[P][s]);
#pragma unroll
            for (int k = 0; k < 4; ++k) v[k] = r < 24 ? v[k] : 0u;
            W[P][s] = as8(v);
        }
    }
    u32x4 ix[8];
#pragma unroll
    for (int u = 0; u < 8; ++u) ix[u] = *(AS3 u32x4*)(lds + L_M + u * 1024 + l16);
    asm volatile("s_waitcnt lgkmcnt(0)\n\ts_barrier" ::: "memory");
    SBAR();
    ST(3);
    issue_x(1);
    issue_m(1);
#pragma unroll
    for (int u = 0; u < 9; ++u) *(AS3 s16x4*)(lds + woa[u]) = wop[u];
    f32x16 bias[2][2];
    {
        AS3 char* tb = lds + L_TAB + wid * 900;
#pragma unroll
        for (int u = 0; u < 8; ++u)
#pragma unroll
            for (int k = 0; k < 4; ++k) {
                bias[u >> 2][(u >> 1) & 1][8 * (u & 1) + 2 * k] = *(AS3 float*)(tb + (ix[u][k] & 0xffffu));
                bias[u >> 2][(u >> 1) & 1][8 * (u & 1) + 2 * k + 1] = *(AS3 float*)(tb + (ix[u][k] >> 16));
            }
    }
    const unsigned ones = ((r & 27) == 24) ? 0x3f803f80u : 0u;
    const int gsw = (r >> 2) & 3;
    const unsigned xa0 = L_X + r * 192 + 16 * (h ^ gsw), xa1 = L_X + r * 192 + 16 * ((h ^ gsw) ^ 2);
    const unsigned owa = L_O + r * OSTR + 48 * wid + 24 * h;
    const unsigned ora = L_O + (16 * wid + 4 * (lane & 3) + ((lane & 15) >> 2)) * OSTR + 16 * (lane >> 4);
    const unsigned l12 = lane * 12;

    auto proj = [&](int i) __attribute__((always_inline)) {
        const unsigned oa = ora + (i & 1) * (64 * OSTR);
        s16x8 a[3];
#pragma unroll
        for (int s = 0; s < 3; ++s) a[s] = *(AS3 s16x8*)(lds + oa + 64 * s);
        f32x4 d[6];
#pragma unroll
        for (int t = 0; t < 6; ++t) {
            d[t] = (f32x4){0.f, 0.f, 0.f, 0.f};
#pragma unroll
            for (int s = 0; s < 3; ++s) d[t] = MFMA16(a[s], *(AS3 s16x8*)(lds + L_WO + (t * 3 + s) * 1024 + l16), d[t]);
        }
        const int wh = win_h(i), ww = win_w(i);
        const int ob = ((8 * wh + 4 + 2 * wid) & 511) * ROWB;
#pragma unroll
        for (int p = 0; p < 4; ++p) {
            const int o = ob + (p >> 1) * ROWB + ((8 * ww + 4 + 4 * (p & 1)) & 511) * 192;
            const u32x3 v = {pk(d[0][p], d[1][p]), pk(d[2][p], d[3][p]), pk(d[4][p], d[5][p])};
            __builtin_amdgcn_raw_buffer_store_b96(v, ro, l12, o, AUX_S);
        }
    };

    ST(4);
    for (int n = 0; n < NIT; ++n) {
        AC(5);
        if (n > 0) proj(n - 1);
        AC(0);
        __builtin_amdgcn_s_setprio(0);
        SBAR();
        u32x4 Q0[2], K0[2], V[2][2];
        u32x2 Q1[2], K1[2];
        const unsigned xo = (n & 1) * 12288;
#pragma unroll
        for (int tt = 0; tt < 2; ++tt) {
            s16x8 X[6];
#pragma unroll
            for (int s = 0; s < 6; ++s) X[s] = *(AS3 s16x8*)(lds + ((s & 1) ? xa1 : xa0) + xo + tt * 6144 + (s >> 1) * 64);
            f32x16 a = {}, c = {}, v = {};
#pragma unroll
            for (int s = 0; s < 6; ++s) a = MFMA32(W[0][s], X[s], a);
#pragma unroll
            for (int s = 0; s < 6; ++s) c = MFMA32(W[1][s], X[s], c);
#pragma unroll
            for (int s = 0; s < 6; ++s) v = MFMA32(X[s], W[2][s], v);
#pragma unroll
            for (int k = 0; k < 4; ++k) Q0[tt][k] = pk(a[2 * k] * QSCALE, a[2 * k + 1] * QSCALE);
#pragma unroll
            for (int k = 0; k < 2; ++k) Q1[tt][k] = pk(a[8 + 2 * k] * QSCALE, a[9 + 2 * k] * QSCALE);
#pragma unroll
            for (int k = 0; k < 4; ++k) K0[tt][k] = pk(c[2 * k], c[2 * k + 1]);
#pragma unroll
            for (int k = 0; k < 2; ++k) K1[tt][k] = pk(c[8 + 2 * k], c[9 + 2 * k]);
#pragma unroll
            for (int k = 0; k < 8; ++k) V[tt][k >> 2][k & 3] = pk(v[2 * k], v[2 * k + 1]) | ones;
            SBAR();
        }
        AC(1);
        __builtin_amdgcn_s_setprio(1);
        const u32x4 fl = *(AS3 u32x4*)(lds + L_FLAG + (n & 1) * 16);
        const bool masked = __builtin_amdgcn_readfirstlane(fl[0] | fl[1] | fl[2] | fl[3]) != 0;
        AS3 char* const ow = lds + owa + (n & 1) * (64 * OSTR);
        bool slow = masked;
        if (!masked) {
            bool bad = false;
#pragma unroll
            for (int qt = 0; qt < 2; ++qt) {
                f32x16 o = {};
#pragma unroll
                for (int kt = 0; kt < 2; ++kt) {
                    f32x16 s = MFMA32(as8(K0[kt]), as8(Q0[qt]), bias[qt][kt]);
                    s = MFMA32K8(as4(K1[kt]), as4(Q1[qt]), s);
#pragma unroll
                    for (int s2 = 0; s2 < 2; ++s2) {
                        u32x4 p;
#pragma unroll
                        for (int k = 0; k < 4; ++k) p[k] = pk(__builtin_amdgcn_exp2f(s[8 * s2 + 2 * k]), __builtin_amdgcn_exp2f(s[8 * s2 + 2 * k + 1]));
                        o = MFMA32(as8(V[kt][s2]), as8(p), o);
                    }
                }
                bad |= !(o[12] >= 0x1p-20f && o[12] <= 0x1p20f);
                put_o(ow + qt * (32 * OSTR), o);
            }
            slow = ANY(bad);
        }
        if (__builtin_expect(slow, 0)) {
            const int ms = (win_h(n) * 64 + win_w(n)) * 8192;
            const int l2 = lane_again();
            const int mv0 = (l2 & 31) * 128 + (l2 >> 5) * 8;
            put_o(ow, exact_tile(K0, K1, V, Q0[0], Q1[0], bias[0], rm, mv0, ms));
            SBAR();
            put_o(ow + 32 * OSTR, exact_tile(K0, K1, V, Q0[1], Q1[1], bias[1], rm, mv0, ms + 4096));
        }
        AC(2);
        if (n + 1 < NIT) {
            if (n == 0) asm volatile("s_waitcnt vmcnt(0)" ::: "memory"); else asm volatile("s_waitcnt vmcnt(4)" ::: "memory");
            AC(3);
            const u32x4 ma = *(AS3 u32x4*)(lds + L_M + wid * 2048 + l16), mb2 = *(AS3 u32x4*)(lds + L_M + wid * 2048 + 1024 + l16);
            put_flag(n + 1, ma, mb2);
        }
        asm volatile("s_waitcnt lgkmcnt(0)\n\ts_barrier" ::: "memory");
        AC(4);
        if (n + 2 < NIT) { issue_x(n + 2); issue_m(n + 2); }
        ST(5 + n);
    }
    proj(NIT - 1);
    ST(13);
}

extern "C" void kernel_launch(void* const* d_in, const int* in_sizes, int n_in, void* d_out, int out_size, void* d_ws, size_t ws_size, hipStream_t stream) {
    swin_kernel<<<NWG, 256, 0, stream>>>((const char*)d_in[0], (const char*)d_in[1], (const unsigned short*)d_in[2], (const u32x2*)d_in[3],
                                         (const i32x4*)d_in[4], (const char*)d_in[5], (char*)d_out
    );
}
```

```cpp
#define PRIO 3
#define ZPAD 1
#define CPF 0
#define TLBW 0
#define AUX_X 0
#define AUX_M 0
#define AUX_S 0
#include <hip/hip_runtime.h>
#include <hip/hip_bf16.h>

typedef __attribute__((ext_vector_type(2))) float f32x2;
typedef __attribute__((ext_vector_type(4))) float f32x4;
typedef __attribute__((ext_vector_type(16))) float f32x16;
typedef __attribute__((ext_vector_type(2))) __bf16 bf2;
typedef __attribute__((ext_vector_type(8))) short s16x8;
typedef __attribute__((ext_vector_type(4))) short s16x4;
typedef __attribute__((ext_vector_type(2))) unsigned u32x2;
typedef __attribute__((ext_vector_type(3))) unsigned u32x3;
typedef __attribute__((ext_vector_type(4))) unsigned u32x4;
typedef __attribute__((ext_vector_type(4))) int i32x4;
#define AS1 __attribute__((address_space(1)))
#define AS3 __attribute__((address_space(3)))
#define DI __device__ __forceinline__
#if defined(__HIP_DEVICE_COMPILE__)
#define NOPK __attribute__((target("no-packed-fp32-ops")))
#else
#define NOPK
#endif

constexpr int ROWB = 512 * 192;
constexpr int OSTR = 208;
constexpr int L_X = 0, L_O = 24576, L_WO = L_O + 2 * 64 * OSTR, L_M = L_WO + 18432, L_TAB = L_M + 8192, L_FLAG = L_TAB + 3600, L_END = L_FLAG + 32;
constexpr int L_STG = 12288;
constexpr int NWG = 512, NIT = 8;
constexpr float LOG2E = 1.4426950408889634f;
constexpr float QSCALE = 0.20412414523193154f * LOG2E;

DI unsigned pk(float a, float b) { f32x2 v = {a, b}; return __builtin_bit_cast(unsigned, __builtin_convertvector(v, bf2)); }
DI s16x8 as8(u32x4 v) { return __builtin_bit_cast(s16x8, v); }
DI s16x4 as4(u32x2 v) { return __builtin_bit_cast(s16x4, v); }
#define MFMA32(a, b, c) __builtin_amdgcn_mfma_f32_32x32x16_bf16((a), (b), (c), 0, 0, 0)
#define MFMA32K8(a, b, c) __builtin_amdgcn_mfma_f32_32x32x8bf16_1k((a), (b), (c), 0, 0, 0)
#define MFMA16(a, b, c) __builtin_amdgcn_mfma_f32_16x16x32_bf16((a), (b), (c), 0, 0, 0)
#define U2F(x) __builtin_bit_cast(float, (unsigned)(x))
#define F2U(x) __builtin_bit_cast(unsigned, (float)(x))
#define ANY(x) (__builtin_amdgcn_ballot_w64(x) != 0)
#define ST(k)
#define AC(k)
#define SBAR() __builtin_amdgcn_sched_barrier(0)

DI int lane_again() { int l; asm volatile("v_mbcnt_lo_u32_b32 %0, -1, 0\n\tv_mbcnt_hi_u32_b32 %0, -1, %0" : "=v"(l)); return l; }
DI f32x16 exact_tile(const u32x4 (&K0)[2], const u32x2 (&K1)[2], const u32x4 (&V)[2][2], u32x4 q0, u32x2 q1, const f32x16 (&bias)[2], __amdgpu_buffer_rsrc_t rm, int mv0, int ms) {
    f32x16 s[2];
    float m = -INFINITY;
#pragma unroll
    for (int kt = 0; kt < 2; ++kt) {
        s[kt] = MFMA32(as8(K0[kt]), as8(q0), bias[kt]);
        s[kt] = MFMA32K8(as4(K1[kt]), as4(q1), s[kt]);
#pragma unroll
        for (int g = 0; g < 4; ++g) {
            const u32x2 mv = __builtin_amdgcn_raw_buffer_load_b64(rm, mv0 + kt * 64 + g * 16, ms, 0);
            s[kt][4 * g + 0] = __builtin_fmaf(U2F(mv[0] << 16), LOG2E, s[kt][4 * g + 0]);
            s[kt][4 * g + 1] = __builtin_fmaf(U2F(mv[0] & 0xffff0000u), LOG2E, s[kt][4 * g + 1]);
            s[kt][4 * g + 2] = __builtin_fmaf(U2F(mv[1] << 16), LOG2E, s[kt][4 * g + 2]);
            s[kt][4 * g + 3] = __builtin_fmaf(U2F(mv[1] & 0xffff0000u), LOG2E, s[kt][4 * g + 3]);
        }
#pragma unroll
        for (int k = 0; k < 16; ++k) m = __builtin_fmaxf(m, s[kt][k]);
        SBAR();
    }
    const auto sw = __builtin_amdgcn_permlane32_swap(F2U(m), F2U(m), false, false);
    m = __builtin_fmaxf(U2F(sw[0]), U2F(sw[1]));
    f32x16 o = {};
#pragma unroll
    for (int kt = 0; kt < 2; ++kt)
#pragma unroll
        for (int s2 = 0; s2 < 2; ++s2) {
            u32x4 p;
#pragma unroll
            for (int k = 0; k < 4; ++k) p[k] = pk(__builtin_amdgcn_exp2f(s[kt][8 * s2 + 2 * k] - m), __builtin_amdgcn_exp2f(s[kt][8 * s2 + 2 * k + 1] - m));
            o = MFMA32(as8(V[kt][s2]), as8(p), o);
        }
    return o;
}

DI void put_o(AS3 char* ow, const f32x16& o) {
    const float ri = __builtin_amdgcn_rcpf(o[12]);
#pragma unroll
    for (int g = 0; g < 3; ++g) *(AS3 u32x2*)(ow + 8 * g) = (u32x2){pk(o[4 * g] * ri, o[4 * g + 1] * ri), pk(o[4 * g + 2] * ri, o[4 * g + 3] * ri)};
}

__global__ __launch_bounds__(256, 2) NOPK void swin_kernel(const char* __restrict__ x, const char* __restrict__ wqkv, const unsigned short* __restrict__ wo,
                                                          const u32x2* __restrict__ rpb, const i32x4* __restrict__ idx, const char* __restrict__ mask,
                                                          char* __restrict__ out
    ) {
    __shared__ __attribute__((aligned(16))) char lds_raw[L_END];
    AS3 char* const lds = (AS3 char*)lds_raw;
    const int tid = __builtin_amdgcn_workitem_id_x(), lane = tid & 63, r = lane & 31, h = lane >> 5;
    const int wid = __builtin_amdgcn_readfirstlane(tid >> 6);
    const int b = __builtin_amdgcn_workgroup_id_x();

    const __amdgpu_buffer_rsrc_t rx = __builtin_amdgcn_make_buffer_rsrc((void*)x, (short)0, 512 * ROWB, 0x00020000);
    const __amdgpu_buffer_rsrc_t rm = __builtin_amdgcn_make_buffer_rsrc((void*)mask, (short)0, 4096 * 8192, 0x00020000);
    const __amdgpu_buffer_rsrc_t ro = __builtin_amdgcn_make_buffer_rsrc((void*)out, (short)0, 512 * ROWB, 0x00020000);
    auto win_h = [&](int i) __attribute__((always_inline)) { return (b >> 6) + 8 * i; };
    auto win_w = [&](int i) __attribute__((always_inline)) { return (b + 8 * i + (b >> 6)) & 63; };

    unsigned rel[3];
#pragma unroll
    for (int k = 0; k < 3; ++k) {
        const int p = 64 * k + lane, nl = p / 12, c = p - 12 * nl;
        rel[k] = (nl >> 3) * ROWB + (nl & 7) * 192 + 16 * (c ^ ((nl >> 2) & 3));
    }
    const unsigned l16 = lane * 16;
    auto issue_x = [&](int i) __attribute__((always_inline)) {
        const int wh = win_h(i), ww = win_w(i);
        const int xb = ((8 * wh + 4 + 2 * wid) & 511) * ROWB + (8 * ww + 4) * 192;
        AS3 char* dst = lds + L_X + (i & 1) * 12288 + wid * 3072;
        if (__builtin_expect(ww == 63, 0)) {
            const int l2 = lane_again();
#pragma unroll
            for (int k = 0; k < 3; ++k) {
                const int nl = (64 * k + l2) / 12;
                const int o = xb + (int)rel[k] - ((nl & 4) ? ROWB : 0);
                __builtin_amdgcn_raw_ptr_buffer_load_lds(rx, (AS3 void*)(dst + k * 1024), 16, o, 0, 0, AUX_X);
            }
        } else {
#pragma unroll
            for (int k = 0; k < 3; ++k)
                __builtin_amdgcn_raw_ptr_buffer_load_lds(rx, (AS3 void*)(dst + k * 1024), 16, rel[k], xb, 0, AUX_X);
        }
    };
    auto issue_m = [&](int i) __attribute__((always_inline)) {
        const int mb = (win_h(i) * 64 + win_w(i)) * 8192 + wid * 2048;
#pragma unroll
        for (int k = 0; k < 2; ++k)
            __builtin_amdgcn_raw_ptr_buffer_load_lds(rm, (AS3 void*)(lds + L_M + wid * 2048 + k * 1024), 16, l16, mb + k * 1024, 0, AUX_M);
    };
    auto put_flag = [&](int i, u32x4 a, u32x4 c) __attribute__((always_inline)) {
        const unsigned o = (a[0] | a[1] | a[2]) | (a[3] | c[0] | c[1]) | (c[2] | c[3]);
        const bool nz = ANY(o != 0);
        if (lane == 0) *(AS3 unsigned*)(lds + L_FLAG + (i & 1) * 16 + wid * 4) = nz ? 1u : 0u;
    };

    ST(0);
    issue_x(0);
    u32x4 m0a, m0b;
    {
        const char* mb = mask + (size_t)(win_h(0) * 64 + win_w(0)) * 8192 + wid * 2048;
        m0a = __builtin_nontemporal_load((const u32x4*)(mb + l16));
        m0b = __builtin_nontemporal_load((const u32x4*)(mb + 1024 + l16));
    }
#pragma unroll
    for (int u = 0; u < 14; ++u) {
        const int q = wid + 4 * u;
        if (q < 54) __builtin_amdgcn_global_load_lds((const AS1 void*)(wqkv + q * 1024 + l16), (AS3 void*)(lds + L_STG + q * 1024), 16, 0, 0);
    }
    i32x4 id4[4];
#pragma unroll
    for (int u = 0; u < 4; ++u) id4[u] = idx[256 * u + tid];
    u32x2 rp = {0u, 0u};
    if (tid < 225) rp = rpb[tid];
    s16x4 wop[9];
    unsigned woa[9];
#pragma unroll
    for (int u = 0; u < 9; ++u) {
        const int id = tid + 256 * u, kq = id / 96, c = id - 96 * kq;
        const int d0 = 24 * (kq / 6) + 8 * (kq % 3) + 4 * ((kq % 6) / 3);
        const unsigned short* p = wo + d0 * 96 + c;
        wop[u] = (s16x4){(short)p[0], (short)p[96], (short)p[192], (short)p[288]};
        const int cl = c / 6, t6 = c - 6 * cl;
        woa[u] = L_WO + (t6 * 3 + (kq >> 3)) * 1024 + (16 * ((kq >> 1) & 3) + cl) * 16 + 8 * (kq & 1);
    }
#pragma unroll
    for (int u = 0; u < 4; ++u) {
        const int i = 16 * u + (tid >> 4), jq = tid & 15;
        int e[4];
#pragma unroll
        for (int k = 0; k < 4; ++k) { const int v = id4[u][k]; e[k] = (v < 0 ? v + 225 : v) * 4; }
        const u32x2 w2 = {(unsigned)e[0] | ((unsigned)e[1] << 16), (unsigned)e[2] | ((unsigned)e[3] << 16)};
        const int qt = i >> 5, kt = jq >> 3, g = (jq >> 1) & 3, hh = jq & 1;
        *(AS3 u32x2*)(lds + L_M + (((qt * 2 + kt) * 2 + (g >> 1)) * 64 + 32 * hh + (i & 31)) * 16 + (g & 1) * 8) = w2;
    }
    if (tid < 225) {
        *(AS3 float*)(lds + L_TAB + 0 * 900 + tid * 4) = U2F(rp[0] << 16) * LOG2E;
        *(AS3 float*)(lds + L_TAB + 1 * 900 + tid * 4) = U2F(rp[0] & 0xffff0000u) * LOG2E;
        *(AS3 float*)(lds + L_TAB + 2 * 900 + tid * 4) = U2F(rp[1] << 16) * LOG2E;
        *(AS3 float*)(lds + L_TAB + 3 * 900 + tid * 4) = U2F(rp[1] & 0xffff0000u) * LOG2E;
    }
    put_flag(0, m0a, m0b);
    ST(1);
    asm volatile("s_waitcnt vmcnt(0) lgkmcnt(0)\n\ts_barrier" ::: "memory");
    ST(2);

    s16x8 W[3][6];
    {
        AS3 char* wb = lds + L_STG + (8 * h + ((lane & 15) >> 2)) * 576 + (24 * wid + 16 * ((lane >> 4) & 1) + 4 * (lane & 3)) * 2;
#pragma unroll
        for (int P = 0; P < 3; ++P)
#pragma unroll
            for (int s = 0; s < 6; ++s) {
                const s16x4 lo = __builtin_amdgcn_ds_read_tr16_b64_v4i16((AS3 s16x4*)(wb + s * 9216 + P * 192));
                const s16x4 hi = __builtin_amdgcn_ds_read_tr16_b64_v4i16((AS3 s16x4*)(wb + s * 9216 + 2304 + P * 192));
                W[P][s] = __builtin_shufflevector(lo, hi, 0, 1, 2, 3, 4, 5, 6, 7);
            }
#pragma unroll
        for (int P = (ZPAD ? 0 : 2); P < 3; ++P)
#pragma unroll
        for (int s = 0; s < 6; ++s) {
            u32x4 v = __builtin_bit_cast(u32x4, W[P][s]);
#pragma unroll
            for (int k = 0; k < 4; ++k) v[k] = r < 24 ? v[k] : 0u;
            W[P][s] = as8(v);
        }
    }
    u32x4 ix[8];
#pragma unroll
    for (int u = 0; u < 8; ++u) ix[u] = *(AS3 u32x4*)(lds + L_M + u * 1024 + l16);
    asm volatile("s_waitcnt lgkmcnt(0)\n\ts_barrier" ::: "memory");
    SBAR();
    ST(3);
    issue_x(1);
    issue_m(1);
#pragma unroll
    for (int u = 0; u < 9; ++u) *(AS3 s16x4*)(lds + woa[u]) = wop[u];
    f32x16 bias[2][2];
    {
        AS3 char* tb = lds + L_TAB + wid * 900;
#pragma unroll
        for (int u = 0; u < 8; ++u)
#pragma unroll
            for (int k = 0; k < 4; ++k) {
                bias[u >> 2][(u >> 1) & 1][8 * (u & 1) + 2 * k] = *(AS3 float*)(tb + (ix[u][k] & 0xffffu));
                bias[u >> 2][(u >> 1) & 1][8 * (u & 1) + 2 * k + 1] = *(AS3 float*)(tb + (ix[u][k] >> 16));
            }
    }
    const unsigned ones = ((r & 27) == 24) ? 0x3f803f80u : 0u;
    const int gsw = (r >> 2) & 3;
    const unsigned xa0 = L_X + r * 192 + 16 * (h ^ gsw), xa1 = L_X + r * 192 + 16 * ((h ^ gsw) ^ 2);
    const unsigned owa = L_O + r * OSTR + 48 * wid + 24 * h;
    const unsigned ora = L_O + (16 * wid + 4 * (lane & 3) + ((lane & 15) >> 2)) * OSTR + 16 * (lane >> 4);
    const unsigned l12 = lane * 12;

    auto proj = [&](int i) __attribute__((always_inline)) {
        const unsigned oa = ora + (i & 1) * (64 * OSTR);
        s16x8 a[3];
#pragma unroll
        for (int s = 0; s < 3; ++s) a[s] = *(AS3 s16x8*)(lds + oa + 64 * s);
        f32x4 d[6];
#pragma unroll
        for (int t = 0; t < 6; ++t) {
            d[t] = (f32x4){0.f, 0.f, 0.f, 0.f};
#pragma unroll
            for (int s = 0; s < 3; ++s) d[t] = MFMA16(a[s], *(AS3 s16x8*)(lds + L_WO + (t * 3 + s) * 1024 + l16), d[t]);
        }
        const int wh = win_h(i), ww = win_w(i);
        const int ob = ((8 * wh + 4 + 2 * wid) & 511) * ROWB;
#pragma unroll
        for (int p = 0; p < 4; ++p) {
            const int o = ob + (p >> 1) * ROWB + ((8 * ww + 4 + 4 * (p & 1)) & 511) * 192;
            const u32x3 v = {pk(d[0][p], d[1][p]), pk(d[2][p], d[3][p]), pk(d[4][p], d[5][p])};
            __builtin_amdgcn_raw_buffer_store_b96(v, ro, l12, o, AUX_S);
        }
    };

    ST(4);
    for (int n = 0; n < NIT; ++n) {
        AC(5);
        if (n > 0) proj(n - 1);
        AC(0);
        __builtin_amdgcn_s_setprio(0);
        SBAR();
        u32x4 Q0[2], K0[2], V[2][2];
        u32x2 Q1[2], K1[2];
        const unsigned xo = (n & 1) * 12288;
#pragma unroll
        for (int tt = 0; tt < 2; ++tt) {
            s16x8 X[6];
#pragma unroll
            for (int s = 0; s < 6; ++s) X[s] = *(AS3 s16x8*)(lds + ((s & 1) ? xa1 : xa0) + xo + tt * 6144 + (s >> 1) * 64);
            f32x16 a = {}, c = {}, v = {};
#pragma unroll
            for (int s = 0; s < 6; ++s) a = MFMA32(W[0][s], X[s], a);
#pragma unroll
            for (int s = 0; s < 6; ++s) c = MFMA32(W[1][s], X[s], c);
#pragma unroll
            for (int s = 0; s < 6; ++s) v = MFMA32(X[s], W[2][s], v);
#pragma unroll
            for (int k = 0; k < 4; ++k) Q0[tt][k] = pk(a[2 * k] * QSCALE, a[2 * k + 1] * QSCALE);
#pragma unroll
            for (int k = 0; k < 2; ++k) Q1[tt][k] = pk(a[8 + 2 * k] * QSCALE, a[9 + 2 * k] * QSCALE);
#pragma unroll
            for (int k = 0; k < 4; ++k) K0[tt][k] = pk(c[2 * k], c[2 * k + 1]);
#pragma unroll
            for (int k = 0; k < 2; ++k) K1[tt][k] = pk(c[8 + 2 * k], c[9 + 2 * k]);
#pragma unroll
            for (int k = 0; k < 8; ++k) V[tt][k >> 2][k & 3] = pk(v[2 * k], v[2 * k + 1]) | ones;
            SBAR();
        }
        AC(1);
        __builtin_amdgcn_s_setprio(1);
        const u32x4 fl = *(AS3 u32x4*)(lds + L_FLAG + (n & 1) * 16);
        const bool masked = __builtin_amdgcn_readfirstlane(fl[0] | fl[1] | fl[2] | fl[3]) != 0;
        AS3 char* const ow = lds + owa + (n & 1) * (64 * OSTR);
        bool slow = masked;
        if (!masked) {
            bool bad = false;
#pragma unroll
            for (int qt = 0; qt < 2; ++qt) {
                f32x16 o = {};
#pragma unroll
                for (int kt = 0; kt < 2; ++kt) {
                    f32x16 s = MFMA32(as8(K0[kt]), as8(Q0[qt]), bias[qt][kt]);
                    s = MFMA32K8(as4(K1[kt]), as4(Q1[qt]), s);
#pragma unroll
                    for (int s2 = 0; s2 < 2; ++s2) {
                        u32x4 p;
#pragma unroll
                        for (int k = 0; k < 4; ++k) p[k] = pk(__builtin_amdgcn_exp2f(s[8 * s2 + 2 * k]), __builtin_amdgcn_exp2f(s[8 * s2 + 2 * k + 1]));
                        o = MFMA32(as8(V[kt][s2]), as8(p), o);
                    }
                }
                bad |= !(o[12] >= 0x1p-20f && o[12] <= 0x1p20f);
                put_o(ow + qt * (32 * OSTR), o);
            }
            slow = ANY(bad);
        }
        if (__builtin_expect(slow, 0)) {
            const int ms = (win_h(n) * 64 + win_w(n)) * 8192;
            const int l2 = lane_again();
            const int mv0 = (l2 & 31) * 128 + (l2 >> 5) * 8;
            put_o(ow, exact_tile(K0, K1, V, Q0[0], Q1[0], bias[0], rm, mv0, ms));
            SBAR();
            put_o(ow + 32 * OSTR, exact_tile(K0, K1, V, Q0[1], Q1[1], bias[1], rm, mv0, ms + 4096));
        }
        AC(2);
        if (n + 1 < NIT) {
            if (n == 0) asm volatile("s_waitcnt vmcnt(0)" ::: "memory"); else asm volatile("s_waitcnt vmcnt(4)" ::: "memory");
            AC(3);
            const u32x4 ma = *(AS3 u32x4*)(lds + L_M + wid * 2048 + l16), mb2 = *(AS3 u32x4*)(lds + L_M + wid * 2048 + 1024 + l16);
            put_flag(n + 1, ma, mb2);
        }
        asm volatile("s_waitcnt lgkmcnt(0)\n\ts_barrier" ::: "memory");
        AC(4);
        if (n + 2 < NIT) { issue_x(n + 2); issue_m(n + 2); }
        ST(5 + n);
    }
    proj(NIT - 1);
    ST(13);
}

__global__ __launch_bounds__(256) void touch_all(const u32x4* __restrict__ x, const u32x4* __restrict__ m, unsigned* __restrict__ ws) {
    size_t b = blockIdx.x; int t = threadIdx.x;
    u32x4 a = x[b * 768 + t] | x[b * 768 + 256 + t] | x[b * 768 + 512 + t] | m[b * 512 + t] | m[b * 512 + 256 + t];
    if ((a[0] ^ a[1] ^ a[2] ^ a[3]) == 0x12345678u) ws[0] = 1;
}
__global__ __launch_bounds__(256) void touch_pages(const unsigned* __restrict__ x, const unsigned* __restrict__ m, const unsigned* __restrict__ o, unsigned* __restrict__ ws) {
    int i = blockIdx.x * 256 + threadIdx.x;
    unsigned a = 0;
    if (i < 12288) a = x[(size_t)i * 1024] ^ o[(size_t)i * 1024];
    if (i < 8192) a ^= m[(size_t)i * 1024];
    if (a == 0x12345678u) ws[0] = 1;
}
__global__ void nothing_k() {}
extern "C" void kernel_launch(void* const* d_in, const int* in_sizes, int n_in, void* d_out, int out_size, void* d_ws, size_t ws_size, hipStream_t stream) {
    swin_kernel<<<NWG, 256, 0, stream>>>((const char*)d_in[0], (const char*)d_in[1], (const unsigned short*)d_in[2], (const u32x2*)d_in[3],
                                         (const i32x4*)d_in[4], (const char*)d_in[5], (char*)d_out
    );
}
```

```cpp
#define PRIO 3
#define ZPAD 1
#define CPF 0
#define TLBW 0
#define AUX_X 0
#define AUX_M 0
#define AUX_S 0
#include <hip/hip_runtime.h>
#include <hip/hip_bf16.h>

typedef __attribute__((ext_vector_type(2))) float f32x2;
typedef __attribute__((ext_vector_type(4))) float f32x4;
typedef __attribute__((ext_vector_type(16))) float f32x16;
typedef __attribute__((ext_vector_type(2))) __bf16 bf2;
typedef __attribute__((ext_vector_type(8))) short s16x8;
typedef __attribute__((ext_vector_type(4))) short s16x4;
typedef __attribute__((ext_vector_type(2))) unsigned u32x2;
typedef __attribute__((ext_vector_type(3))) unsigned u32x3;
typedef __attribute__((ext_vector_type(4))) unsigned u32x4;
typedef __attribute__((ext_vector_type(4))) int i32x4;
#define AS1 __attribute__((address_space(1)))
#define AS3 __attribute__((address_space(3)))
#define DI __device__ __forceinline__
#if defined(__HIP_DEVICE_COMPILE__)
#define NOPK __attribute__((target("no-packed-fp32-ops")))
#else
#define NOPK
#endif

constexpr int ROWB = 512 * 192;
constexpr int OSTR = 208;
constexpr int L_X = 0, L_O = 24576, L_WO = L_O + 2 * 64 * OSTR, L_M = L_WO + 18432, L_TAB = L_M + 8192, L_FLAG = L_TAB + 3600, L_END = L_FLAG + 32;
constexpr int L_STG = 12288;
constexpr int NWG = 512, NIT = 8;
constexpr float LOG2E = 1.4426950408889634f;
constexpr float QSCALE = 0.20412414523193154f * LOG2E;

DI unsigned pk(float a, float b) { f32x2 v = {a, b}; return __builtin_bit_cast(unsigned, __builtin_convertvector(v, bf2)); }
DI s16x8 as8(u32x4 v) { return __builtin_bit_cast(s16x8, v); }
DI s16x4 as4(u32x2 v) { return __builtin_bit_cast(s16x4, v); }
#define MFMA32(a, b, c) __builtin_amdgcn_mfma_f32_32x32x16_bf16((a), (b), (c), 0, 0, 0)
#define MFMA32K8(a, b, c) __builtin_amdgcn_mfma_f32_32x32x8bf16_1k((a), (b), (c), 0, 0, 0)
#define MFMA16(a, b, c) __builtin_amdgcn_mfma_f32_16x16x32_bf16((a), (b), (c), 0, 0, 0)
#define U2F(x) __builtin_bit_cast(float, (unsigned)(x))
#define F2U(x) __builtin_bit_cast(unsigned, (float)(x))
#define ANY(x) (__builtin_amdgcn_ballot_w64(x) != 0)
#define ST(k)
#define AC(k)
#define SBAR() __builtin_amdgcn_sched_barrier(0)

DI int lane_again() { int l; asm volatile("v_mbcnt_lo_u32_b32 %0, -1, 0\n\tv_mbcnt_hi_u32_b32 %0, -1, %0" : "=v"(l)); return l; }
DI f32x16 exact_tile(const u32x4 (&K0)[2], const u32x2 (&K1)[2], const u32x4 (&V)[2][2], u32x4 q0, u32x2 q1, const f32x16 (&bias)[2], __amdgpu_buffer_rsrc_t rm, int mv0, int ms) {
    f32x16 s[2];
    float m = -INFINITY;
#pragma unroll
    for (int kt = 0; kt < 2; ++kt) {
        s[kt] = MFMA32(as8(K0[kt]), as8(q0), bias[kt]);
        s[kt] = MFMA32K8(as4(K1[kt]), as4(q1), s[kt]);
#pragma unroll
        for (int g = 0; g < 4; ++g) {
            const u32x2 mv = __builtin_amdgcn_raw_buffer_load_b64(rm, mv0 + kt * 64 + g * 16, ms, 0);
            s[kt][4 * g + 0] = __builtin_fmaf(U2F(mv[0] << 16), LOG2E, s[kt][4 * g + 0]);
            s[kt][4 * g + 1] = __builtin_fmaf(U2F(mv[0] & 0xffff0000u), LOG2E, s[kt][4 * g + 1]);
            s[kt][4 * g + 2] = __builtin_fmaf(U2F(mv[1] << 16), LOG2E, s[kt][4 * g + 2]);
            s[kt][4 * g + 3] = __builtin_fmaf(U2F(mv[1] & 0xffff0000u), LOG2E, s[kt][4 * g + 3]);
        }
#pragma unroll
        for (int k = 0; k < 16; ++k) m = __builtin_fmaxf(m, s[kt][k]);
        SBAR();
    }
    const auto sw = __builtin_amdgcn_permlane32_swap(F2U(m), F2U(m), false, false);
    m = __builtin_fmaxf(U2F(sw[0]), U2F(sw[1]));
    f32x16 o = {};
#pragma unroll
    for (int kt = 0; kt < 2; ++kt)
#pragma unroll
        for (int s2 = 0; s2 < 2; ++s2) {
            u32x4 p;
#pragma unroll
            for (int k = 0; k < 4; ++k) p[k] = pk(__builtin_amdgcn_exp2f(s[kt][8 * s2 + 2 * k] - m), __builtin_amdgcn_exp2f(s[kt][8 * s2 + 2 * k + 1] - m));
            o = MFMA32(as8(V[kt][s2]), as8(p), o);
        }
    return o;
}

DI void put_o(AS3 char* ow, const f32x16& o) {
    const float ri = __builtin_amdgcn_rcpf(o[12]);
#pragma unroll
    for (int g = 0; g < 3; ++g) *(AS3 u32x2*)(ow + 8 * g) = (u32x2){pk(o[4 * g] * ri, o[4 * g + 1] * ri), pk(o[4 * g + 2] * ri, o[4 * g + 3] * ri)};
}

__global__ __launch_bounds__(256, 2) NOPK void swin_kernel(const char* __restrict__ x, const char* __restrict__ wqkv, const unsigned short* __restrict__ wo,
                                                          const u32x2* __restrict__ rpb, const i32x4* __restrict__ idx, const char* __restrict__ mask,
                                                          char* __restrict__ out
    ) {
    __shared__ __attribute__((aligned(16))) char lds_raw[L_END];
    AS3 char* const lds = (AS3 char*)lds_raw;
    const int tid = __builtin_amdgcn_workitem_id_x(), lane = tid & 63, r = lane & 31, h = lane >> 5;
    const int wid = __builtin_amdgcn_readfirstlane(tid >> 6);
    const int b = __builtin_amdgcn_workgroup_id_x();

    const __amdgpu_buffer_rsrc_t rx = __builtin_amdgcn_make_buffer_rsrc((void*)x, (short)0, 512 * ROWB, 0x00020000);
    const __amdgpu_buffer_rsrc_t rm = __builtin_amdgcn_make_buffer_rsrc((void*)mask, (short)0, 4096 * 8192, 0x00020000);
    const __amdgpu_buffer_rsrc_t ro = __builtin_amdgcn_make_buffer_rsrc((void*)out, (short)0, 512 * ROWB, 0x00020000);
    auto win_h = [&](int i) __attribute__((always_inline)) { return (b >> 6) + 8 * i; };
    auto win_w = [&](int i) __attribute__((always_inline)) { return (b + 8 * i + (b >> 6)) & 63; };

    unsigned rel[3];
#pragma unroll
    for (int k = 0; k < 3; ++k) {
        const int p = 64 * k + lane, nl = p / 12, c = p - 12 * nl;
        rel[k] = (nl >> 3) * ROWB + (nl & 7) * 192 + 16 * (c ^ ((nl >> 2) & 3));
    }
    const unsigned l16 = lane * 16;
    auto issue_x = [&](int i) __attribute__((always_inline)) {
        const int wh = win_h(i), ww = win_w(i);
        const int xb = ((8 * wh + 4 + 2 * wid) & 511) * ROWB + (8 * ww + 4) * 192;
        AS3 char* dst = lds + L_X + (i & 1) * 12288 + wid * 3072;
        if (__builtin_expect(ww == 63, 0)) {
            const int l2 = lane_again();
#pragma unroll
            for (int k = 0; k < 3; ++k) {
                const int nl = (64 * k + l2) / 12;
                const int o = xb + (int)rel[k] - ((nl & 4) ? ROWB : 0);
                __builtin_amdgcn_raw_ptr_buffer_load_lds(rx, (AS3 void*)(dst + k * 1024), 16, o, 0, 0, AUX_X);
            }
        } else {
#pragma unroll
            for (int k = 0; k < 3; ++k)
                __builtin_amdgcn_raw_ptr_buffer_load_lds(rx, (AS3 void*)(dst + k * 1024), 16, rel[k], xb, 0, AUX_X);
        }
    };
    auto issue_m = [&](int i) __attribute__((always_inline)) {
        const int mb = (win_h(i) * 64 + win_w(i)) * 8192 + wid * 2048;
#pragma unroll
        for (int k = 0; k < 2; ++k)
            __builtin_amdgcn_raw_ptr_buffer_load_lds(rm, (AS3 void*)(lds + L_M + wid * 2048 + k * 1024), 16, l16, mb + k * 1024, 0, AUX_M);
    };
    auto put_flag = [&](int i, u32x4 a, u32x4 c) __attribute__((always_inline)) {
        const unsigned o = (a[0] | a[1] | a[2]) | (a[3] | c[0] | c[1]) | (c[2] | c[3]);
        const bool nz = ANY(o != 0);
        if (lane == 0) *(AS3 unsigned*)(lds + L_FLAG + (i & 1) * 16 + wid * 4) = nz ? 1u : 0u;
    };

    { unsigned d_; asm volatile(".rept 1\n\ts_mov_b32 %0, 0x12345678\n\t.endr" : "=s"(d_)); }
    ST(0);
    issue_x(0);
    u32x4 m0a, m0b;
    {
        const char* mb = mask + (size_t)(win_h(0) * 64 + win_w(0)) * 8192 + wid * 2048;
        m0a = __builtin_nontemporal_load((const u32x4*)(mb + l16));
        m0b = __builtin_nontemporal_load((const u32x4*)(mb + 1024 + l16));
    }
#pragma unroll
    for (int u = 0; u < 14; ++u) {
        const int q = wid + 4 * u;
        if (q < 54) __builtin_amdgcn_global_load_lds((const AS1 void*)(wqkv + q * 1024 + l16), (AS3 void*)(lds + L_STG + q * 1024), 16, 0, 0);
    }
    i32x4 id4[4];
#pragma unroll
    for (int u = 0; u < 4; ++u) id4[u] = idx[256 * u + tid];
    u32x2 rp = {0u, 0u};
    if (tid < 225) rp = rpb[tid];
    s16x4 wop[9];
    unsigned woa[9];
#pragma unroll
    for (int u = 0; u < 9; ++u) {
        const int id = tid + 256 * u, kq = id / 96, c = id - 96 * kq;
        const int d0 = 24 * (kq / 6) + 8 * (kq % 3) + 4 * ((kq % 6) / 3);
        const unsigned short* p = wo + d0 * 96 + c;
        wop[u] = (s16x4){(short)p[0], (short)p[96], (short)p[192], (short)p[288]};
        const int cl = c / 6, t6 = c - 6 * cl;
        woa[u] = L_WO + (t6 * 3 + (kq >> 3)) * 1024 + (16 * ((kq >> 1) & 3) + cl) * 16 + 8 * (kq & 1);
    }
#pragma unroll
    for (int u = 0; u < 4; ++u) {
        const int i = 16 * u + (tid >> 4), jq = tid & 15;
        int e[4];
#pragma unroll
        for (int k = 0; k < 4; ++k) { const int v = id4[u][k]; e[k] = (v < 0 ? v + 225 : v) * 4; }
        const u32x2 w2 = {(unsigned)e[0] | ((unsigned)e[1] << 16), (unsigned)e[2] | ((unsigned)e[3] << 16)};
        const int qt = i >> 5, kt = jq >> 3, g = (jq >> 1) & 3, hh = jq & 1;
        *(AS3 u32x2*)(lds + L_M + (((qt * 2 + kt) * 2 + (g >> 1)) * 64 + 32 * hh + (i & 31)) * 16 + (g & 1) * 8) = w2;
    }
    if (tid < 225) {
        *(AS3 float*)(lds + L_TAB + 0 * 900 + tid * 4) = U2F(rp[0] << 16) * LOG2E;
        *(AS3 float*)(lds + L_TAB + 1 * 900 + tid * 4) = U2F(rp[0] & 0xffff0000u) * LOG2E;
        *(AS3 float*)(lds + L_TAB + 2 * 900 + tid * 4) = U2F(rp[1] << 16) * LOG2E;
        *(AS3 float*)(lds + L_TAB + 3 * 900 + tid * 4) = U2F(rp[1] & 0xffff0000u) * LOG2E;
    }
    put_flag(0, m0a, m0b);
    ST(1);
    asm volatile("s_waitcnt vmcnt(0) lgkmcnt(0)\n\ts_barrier" ::: "memory");
    ST(2);

    s16x8 W[3][6];
    {
        AS3 char* wb = lds + L_STG + (8 * h + ((lane & 15) >> 2)) * 576 + (24 * wid + 16 * ((lane >> 4) & 1) + 4 * (lane & 3)) * 2;
#pragma unroll
        for (int P = 0; P < 3; ++P)
#pragma unroll
            for (int s = 0; s < 6; ++s) {
                const s16x4 lo = __builtin_amdgcn_ds_read_tr16_b64_v4i16((AS3 s16x4*)(wb + s * 9216 + P * 192));
                const s16x4 hi = __builtin_amdgcn_ds_read_tr16_b64_v4i16((AS3 s16x4*)(wb + s * 9216 + 2304 + P * 192));
                W[P][s] = __builtin_shufflevector(lo, hi, 0, 1, 2, 3, 4, 5, 6, 7);
            }
#pragma unroll
        for (int P = (ZPAD ? 0 : 2); P < 3; ++P)
#pragma unroll
        for (int s = 0; s < 6; ++s) {
            u32x4 v = __builtin_bit_cast(u32x4, W[P][s]);
#pragma unroll
            for (int k = 0; k < 4; ++k) v[k] = r < 24 ? v[k] : 0u;
            W[P][s] = as8(v);
        }
    }
    u32x4 ix[8];
#pragma unroll
    for (int u = 0; u < 8; ++u) ix[u] = *(AS3 u32x4*)(lds + L_M + u * 1024 + l16);
    asm volatile("s_waitcnt lgkmcnt(0)\n\ts_barrier" ::: "memory");
    SBAR();
    ST(3);
    issue_x(1);
    issue_m(1);
#pragma unroll
    for (int u = 0; u < 9; ++u) *(AS3 s16x4*)(lds + woa[u]) = wop[u];
    f32x16 bias[2][2];
    {
        AS3 char* tb = lds + L_TAB + wid * 900;
#pragma unroll
        for (int u = 0; u < 8; ++u)
#pragma unroll
            for (int k = 0; k < 4; ++k) {
                bias[u >> 2][(u >> 1) & 1][8 * (u & 1) + 2 * k] = *(AS3 float*)(tb + (ix[u][k] & 0xffffu));
                bias[u >> 2][(u >> 1) & 1][8 * (u & 1) + 2 * k + 1] = *(AS3 float*)(tb + (ix[u][k] >> 16));
            }
    }
    const unsigned ones = ((r & 27) == 24) ? 0x3f803f80u : 0u;
    const int gsw = (r >> 2) & 3;
    const unsigned xa0 = L_X + r * 192 + 16 * (h ^ gsw), xa1 = L_X + r * 192 + 16 * ((h ^ gsw) ^ 2);
    const unsigned owa = L_O + r * OSTR + 48 * wid + 24 * h;
    const unsigned ora = L_O + (16 * wid + 4 * (lane & 3) + ((lane & 15) >> 2)) * OSTR + 16 * (lane >> 4);
    const unsigned l12 = lane * 12;

    auto proj = [&](int i) __attribute__((always_inline)) {
        const unsigned oa = ora + (i & 1) * (64 * OSTR);
        s16x8 a[3];
#pragma unroll
        for (int s = 0; s < 3; ++s) a[s] = *(AS3 s16x8*)(lds + oa + 64 * s);
        f32x4 d[6];
#pragma unroll
        for (int t = 0; t < 6; ++t) {
            d[t] = (f32x4){0.f, 0.f, 0.f, 0.f};
#pragma unroll
            for (int s = 0; s < 3; ++s) d[t] = MFMA16(a[s], *(AS3 s16x8*)(lds + L_WO + (t * 3 + s) * 1024 + l16), d[t]);
        }
        const int wh = win_h(i), ww = win_w(i);
        const int ob = ((8 * wh + 4 + 2 * wid) & 511) * ROWB;
#pragma unroll
        for (int p = 0; p < 4; ++p) {
            const int o = ob + (p >> 1) * ROWB + ((8 * ww + 4 + 4 * (p & 1)) & 511) * 192;
            const u32x3 v = {pk(d[0][p], d[1][p]), pk(d[2][p], d[3][p]), pk(d[4][p], d[5][p])};
            __builtin_amdgcn_raw_buffer_store_b96(v, ro, l12, o, AUX_S);
        }
    };

    ST(4);
    for (int n = 0; n < NIT; ++n) {
        AC(5);
        if (n > 0) proj(n - 1);
        AC(0);
        __builtin_amdgcn_s_setprio(0);
        SBAR();
        u32x4 Q0[2], K0[2], V[2][2];
        u32x2 Q1[2], K1[2];
        const unsigned xo = (n & 1) * 12288;
#pragma unroll
        for (int tt = 0; tt < 2; ++tt) {
            s16x8 X[6];
#pragma unroll
            for (int s = 0; s < 6; ++s) X[s] = *(AS3 s16x8*)(lds + ((s & 1) ? xa1 : xa0) + xo + tt * 6144 + (s >> 1) * 64);
            f32x16 a = {}, c = {}, v = {};
#pragma unroll
            for (int s = 0; s < 6; ++s) a = MFMA32(W[0][s], X[s], a);
#pragma unroll
            for (int s = 0; s < 6; ++s) c = MFMA32(W[1][s], X[s], c);
#pragma unroll
            for (int s = 0; s < 6; ++s) v = MFMA32(X[s], W[2][s], v);
#pragma unroll
            for (int k = 0; k < 4; ++k) Q0[tt][k] = pk(a[2 * k] * QSCALE, a[2 * k + 1] * QSCALE);
#pragma unroll
            for (int k = 0; k < 2; ++k) Q1[tt][k] = pk(a[8 + 2 * k] * QSCALE, a[9 + 2 * k] * QSCALE);
#pragma unroll
            for (int k = 0; k < 4; ++k) K0[tt][k] = pk(c[2 * k], c[2 * k + 1]);
#pragma unroll
            for (int k = 0; k < 2; ++k) K1[tt][k] = pk(c[8 + 2 * k], c[9 + 2 * k]);
#pragma unroll
            for (int k = 0; k < 8; ++k) V[tt][k >> 2][k & 3] = pk(v[2 * k], v[2 * k + 1]) | ones;
            SBAR();
        }
        AC(1);
        __builtin_amdgcn_s_setprio(1);
        const u32x4 fl = *(AS3 u32x4*)(lds + L_FLAG + (n & 1) * 16);
        const bool masked = __builtin_amdgcn_readfirstlane(fl[0] | fl[1] | fl[2] | fl[3]) != 0;
        AS3 char* const ow = lds + owa + (n & 1) * (64 * OSTR);
        bool slow = masked;
        if (!masked) {
            bool bad = false;
#pragma unroll
            for (int qt = 0; qt < 2; ++qt) {
                f32x16 o = {};
#pragma unroll
                for (int kt = 0; kt < 2; ++kt) {
                    f32x16 s = MFMA32(as8(K0[kt]), as8(Q0[qt]), bias[qt][kt]);
                    s = MFMA32K8(as4(K1[kt]), as4(Q1[qt]), s);
#pragma unroll
                    for (int s2 = 0; s2 < 2; ++s2) {
                        u32x4 p;
#pragma unroll
                        for (int k = 0; k < 4; ++k) p[k] = pk(__builtin_amdgcn_exp2f(s[8 * s2 + 2 * k]), __builtin_amdgcn_exp2f(s[8 * s2 + 2 * k + 1]));
                        o = MFMA32(as8(V[kt][s2]), as8(p), o);
                    }
                }
                bad |= !(o[12] >= 0x1p-20f && o[12] <= 0x1p20f);
                put_o(ow + qt * (32 * OSTR), o);
            }
            slow = ANY(bad);
        }
        if (__builtin_expect(slow, 0)) {
            const int ms = (win_h(n) * 64 + win_w(n)) * 8192;
            const int l2 = lane_again();
            const int mv0 = (l2 & 31) * 128 + (l2 >> 5) * 8;
            put_o(ow, exact_tile(K0, K1, V, Q0[0], Q1[0], bias[0], rm, mv0, ms));
            SBAR();
            put_o(ow + 32 * OSTR, exact_tile(K0, K1, V, Q0[1], Q1[1], bias[1], rm, mv0, ms + 4096));
        }
        AC(2);
        if (n + 1 < NIT) {
            if (n == 0) asm volatile("s_waitcnt vmcnt(0)" ::: "memory"); else asm volatile("s_waitcnt vmcnt(4)" ::: "memory");
            AC(3);
            const u32x4 ma = *(AS3 u32x4*)(lds + L_M + wid * 2048 + l16), mb2 = *(AS3 u32x4*)(lds + L_M + wid * 2048 + 1024 + l16);
            put_flag(n + 1, ma, mb2);
        }
        asm volatile("s_waitcnt lgkmcnt(0)\n\ts_barrier" ::: "memory");
        AC(4);
        if (n + 2 < NIT) { issue_x(n + 2); issue_m(n + 2); }
        ST(5 + n);
    }
    proj(NIT - 1);
    ST(13);
}

extern "C" void kernel_launch(void* const* d_in, const int* in_sizes, int n_in, void* d_out, int out_size, void* d_ws, size_t ws_size, hipStream_t stream) {
    swin_kernel<<<NWG, 256, 0, stream>>>((const char*)d_in[0], (const char*)d_in[1], (const unsigned short*)d_in[2], (const u32x2*)d_in[3],
                                         (const i32x4*)d_in[4], (const char*)d_in[5], (char*)d_out
    );
}
```

```cpp
#include <hip/hip_runtime.h>
#include <hip/hip_bf16.h>
typedef __hip_bfloat16 bf16;
__device__ __forceinline__ float b2f(unsigned short u){ return __uint_as_float(((unsigned)u)<<16); }
__device__ __forceinline__ unsigned short f2b(float f){ unsigned u=__float_as_uint(f); u += 0x7fffu + ((u>>16)&1u); return (unsigned short)(u>>16); }
__global__ __launch_bounds__(256) void naive_k(const unsigned short* x, const unsigned short* wqkv, const unsigned short* wo, const unsigned short* rpb, const int* idx, const unsigned short* mask, unsigned short* out){
  __shared__ float xs[64*96];
  __shared__ float qkv[64*288];
  __shared__ float s[64*65];
  int w = blockIdx.x, wh = w>>6, ww = w&63, t = threadIdx.x;
  for(int e=t;e<64*96;e+=256){ int n=e/96,c=e%96; int r=(wh*8+(n>>3)+4)&511, cc=(ww*8+(n&7)+4)&511; xs[e]=b2f(x[((size_t)r*512+cc)*96+c]); }
  __syncthreads();
  for(int e=t;e<64*288;e+=256){ int n=e/288,d=e%288; float a=0; for(int c=0;c<96;++c) a+=xs[n*96+c]*b2f(wqkv[c*288+d]); qkv[e]=b2f(f2b(a)); }
  __syncthreads();
  for(int h=0;h<4;++h){
    for(int e=t;e<4096;e+=256){ int i=e>>6,j=e&63; float a=0; for(int d=0;d<24;++d) a+=qkv[i*288+h*24+d]*qkv[j*288+96+h*24+d];
      a = a*0.20412414523193154f + b2f(rpb[idx[e]*4+h]) + b2f(mask[(size_t)w*4096+e]); s[i*65+j]=a; }
    __syncthreads();
    if(t<64){ float m=-INFINITY; for(int j=0;j<64;++j) m=fmaxf(m,s[t*65+j]); float l=0; for(int j=0;j<64;++j){ float p=__expf(s[t*65+j]-m); s[t*65+j]=p; l+=p;} float r=1.f/l; for(int j=0;j<64;++j) s[t*65+j]=b2f(f2b(s[t*65+j]*r)); }
    __syncthreads();
    for(int e=t;e<64*24;e+=256){ int i=e/24,d=e%24; float a=0; for(int j=0;j<64;++j) a+=s[i*65+j]*qkv[j*288+192+h*24+d]; xs[i*96+h*24+d]=b2f(f2b(a)); }
    __syncthreads();
  }
  for(int e=t;e<64*96;e+=256){ int n=e/96,c=e%96; float a=0; for(int d=0;d<96;++d) a+=xs[n*96+d]*b2f(wo[d*96+c]);
    int r=(wh*8+(n>>3)+4)&511, cc=(ww*8+(n&7)+4)&511; out[((size_t)r*512+cc)*96+c]=f2b(a); }
}

typedef __attribute__((ext_vector_type(4))) unsigned u32x4;
__global__ __launch_bounds__(256) void touch_all(const u32x4* __restrict__ x, const u32x4* __restrict__ m, unsigned* __restrict__ ws) {
    size_t b = blockIdx.x; int t = threadIdx.x;
    u32x4 a = x[b * 768 + t] | x[b * 768 + 256 + t] | x[b * 768 + 512 + t] | m[b * 512 + t] | m[b * 512 + 256 + t];
    if ((a[0] ^ a[1] ^ a[2] ^ a[3]) == 0x12345678u) ws[0] = 1;
}
extern "C" void kernel_launch(void* const* d_in, const int* in_sizes, int n_in, void* d_out, int out_size, void* d_ws, size_t ws_size, hipStream_t stream) {
  touch_all<<<4096,256,0,stream>>>((const u32x4*)d_in[0],(const u32x4*)d_in[5],(unsigned*)d_ws);
  naive_k<<<4096,256,0,stream>>>((const unsigned short*)d_in[0],(const unsigned short*)d_in[1],(const unsigned short*)d_in[2],(const unsigned short*)d_in[3],(const int*)d_in[4],(const unsigned short*)d_in[5],(unsigned short*)d_out);
}
```

```cpp
#define PRIO 3
#define ZPAD 1
#define CPF 0
#define TLBW 0
#define AUX_X 0
#define AUX_M 0
#define AUX_S 0
#include <hip/hip_runtime.h>
#include <hip/hip_bf16.h>

typedef __attribute__((ext_vector_type(2))) float f32x2;
typedef __attribute__((ext_vector_type(4))) float f32x4;
typedef __attribute__((ext_vector_type(16))) float f32x16;
typedef __attribute__((ext_vector_type(2))) __bf16 bf2;
typedef __attribute__((ext_vector_type(8))) short s16x8;
typedef __attribute__((ext_vector_type(4))) short s16x4;
typedef __attribute__((ext_vector_type(2))) unsigned u32x2;
typedef __attribute__((ext_vector_type(3))) unsigned u32x3;
typedef __attribute__((ext_vector_type(4))) unsigned u32x4;
typedef __attribute__((ext_vector_type(4))) int i32x4;
#define AS1 __attribute__((address_space(1)))
#define AS3 __attribute__((address_space(3)))
#define DI __device__ __forceinline__
#if defined(__HIP_DEVICE_COMPILE__)
#define NOPK __attribute__((target("no-packed-fp32-ops")))
#else
#define NOPK
#endif

constexpr int ROWB = 512 * 192;
constexpr int OSTR = 208;
constexpr int L_X = 0, L_O = 24576, L_WO = L_O + 2 * 64 * OSTR, L_M = L_WO + 18432, L_TAB = L_M + 8192, L_FLAG = L_TAB + 3600, L_END = L_FLAG + 32;
constexpr int L_STG = 12288;
constexpr int NWG = 512, NIT = 8;
constexpr float LOG2E = 1.4426950408889634f;
constexpr float QSCALE = 0.20412414523193154f * LOG2E;

DI unsigned pk(float a, float b) { f32x2 v = {a, b}; return __builtin_bit_cast(unsigned, __builtin_convertvector(v, bf2)); }
DI s16x8 as8(u32x4 v) { return __builtin_bit_cast(s16x8, v); }
DI s16x4 as4(u32x2 v) { return __builtin_bit_cast(s16x4, v); }
#define MFMA32(a, b, c) __builtin_amdgcn_mfma_f32_32x32x16_bf16((a), (b), (c), 0, 0, 0)
#define MFMA32K8(a, b, c) __builtin_amdgcn_mfma_f32_32x32x8bf16_1k((a), (b), (c), 0, 0, 0)
#define MFMA16(a, b, c) __builtin_amdgcn_mfma_f32_16x16x32_bf16((a), (b), (c), 0, 0, 0)
#define U2F(x) __builtin_bit_cast(float, (unsigned)(x))
#define F2U(x) __builtin_bit_cast(unsigned, (float)(x))
#define ANY(x) (__builtin_amdgcn_ballot_w64(x) != 0)
#define ST(k)
#define AC(k)
#define SBAR() __builtin_amdgcn_sched_barrier(0)

DI int lane_again() { int l; asm volatile("v_mbcnt_lo_u32_b32 %0, -1, 0\n\tv_mbcnt_hi_u32_b32 %0, -1, %0" : "=v"(l)); return l; }
DI f32x16 exact_tile(const u32x4 (&K0)[2], const u32x2 (&K1)[2], const u32x4 (&V)[2][2], u32x4 q0, u32x2 q1, const f32x16 (&bias)[2], __amdgpu_buffer_rsrc_t rm, int mv0, int ms) {
    f32x16 s[2];
    float m = -INFINITY;
#pragma unroll
    for (int kt = 0; kt < 2; ++kt) {
        s[kt] = MFMA32(as8(K0[kt]), as8(q0), bias[kt]);
        s[kt] = MFMA32K8(as4(K1[kt]), as4(q1), s[kt]);
#pragma unroll
        for (int g = 0; g < 4; ++g) {
            const u32x2 mv = __builtin_amdgcn_raw_buffer_load_b64(rm, mv0 + kt * 64 + g * 16, ms, 0);
            s[kt][4 * g + 0] = __builtin_fmaf(U2F(mv[0] << 16), LOG2E, s[kt][4 * g + 0]);
            s[kt][4 * g + 1] = __builtin_fmaf(U2F(mv[0] & 0xffff0000u), LOG2E, s[kt][4 * g + 1]);
            s[kt][4 * g + 2] = __builtin_fmaf(U2F(mv[1] << 16), LOG2E, s[kt][4 * g + 2]);
            s[kt][4 * g + 3] = __builtin_fmaf(U2F(mv[1] & 0xffff0000u), LOG2E, s[kt][4 * g + 3]);
        }
#pragma unroll
        for (int k = 0; k < 16; ++k) m = __builtin_fmaxf(m, s[kt][k]);
        SBAR();
    }
    const auto sw = __builtin_amdgcn_permlane32_swap(F2U(m), F2U(m), false, false);
    m = __builtin_fmaxf(U2F(sw[0]), U2F(sw[1]));
    f32x16 o = {};
#pragma unroll
    for (int kt = 0; kt < 2; ++kt)
#pragma unroll
        for (int s2 = 0; s2 < 2; ++s2) {
            u32x4 p;
#pragma unroll
            for (int k = 0; k < 4; ++k) p[k] = pk(__builtin_amdgcn_exp2f(s[kt][8 * s2 + 2 * k] - m), __builtin_amdgcn_exp2f(s[kt][8 * s2 + 2 * k + 1] - m));
            o = MFMA32(as8(V[kt][s2]), as8(p), o);
        }
    return o;
}

DI void put_o(AS3 char* ow, const f32x16& o) {
    const float ri = __builtin_amdgcn_rcpf(o[12]);
#pragma unroll
    for (int g = 0; g < 3; ++g) *(AS3 u32x2*)(ow + 8 * g) = (u32x2){pk(o[4 * g] * ri, o[4 * g + 1] * ri), pk(o[4 * g + 2] * ri, o[4 * g + 3] * ri)};
}

__global__ __launch_bounds__(256, 2) NOPK void swin_kernel(const char* __restrict__ x, const char* __restrict__ wqkv, const unsigned short* __restrict__ wo,
                                                          const u32x2* __restrict__ rpb, const i32x4* __restrict__ idx, const char* __restrict__ mask,
                                                          char* __restrict__ out
    ) {
    __shared__ __attribute__((aligned(16))) char lds_raw[L_END];
    AS3 char* const lds = (AS3 char*)lds_raw;
    const int tid = __builtin_amdgcn_workitem_id_x(), lane = tid & 63, r = lane & 31, h = lane >> 5;
    const int wid = __builtin_amdgcn_readfirstlane(tid >> 6);
    const int b = __builtin_amdgcn_workgroup_id_x();

    const __amdgpu_buffer_rsrc_t rx = __builtin_amdgcn_make_buffer_rsrc((void*)x, (short)0, 512 * ROWB, 0x00020000);
    const __amdgpu_buffer_rsrc_t rm = __builtin_amdgcn_make_buffer_rsrc((void*)mask, (short)0, 4096 * 8192, 0x00020000);
    const __amdgpu_buffer_rsrc_t ro = __builtin_amdgcn_make_buffer_rsrc((void*)out, (short)0, 512 * ROWB, 0x00020000);
    auto win_h = [&](int i) __attribute__((always_inline)) { return (b >> 6) + 8 * i; };
    auto win_w = [&](int i) __attribute__((always_inline)) { return (b + 8 * i + (b >> 6)) & 63; };

    unsigned rel[3];
#pragma unroll
    for (int k = 0; k < 3; ++k) {
        const int p = 64 * k + lane, nl = p / 12, c = p - 12 * nl;
        rel[k] = (nl >> 3) * ROWB + (nl & 7) * 192 + 16 * (c ^ ((nl >> 2) & 3));
    }
    const unsigned l16 = lane * 16;
    auto issue_x = [&](int i) __attribute__((always_inline)) {
        const int wh = win_h(i), ww = win_w(i);
        const int xb = ((8 * wh + 4 + 2 * wid) & 511) * ROWB + (8 * ww + 4) * 192;
        AS3 char* dst = lds + L_X + (i & 1) * 12288 + wid * 3072;
        if (__builtin_expect(ww == 63, 0)) {
            const int l2 = lane_again();
#pragma unroll
            for (int k = 0; k < 3; ++k) {
                const int nl = (64 * k + l2) / 12;
                const int o = xb + (int)rel[k] - ((nl & 4) ? ROWB : 0);
                __builtin_amdgcn_raw_ptr_buffer_load_lds(rx, (AS3 void*)(dst + k * 1024), 16, o, 0, 0, AUX_X);
            }
        } else {
#pragma unroll
            for (int k = 0; k < 3; ++k)
                __builtin_amdgcn_raw_ptr_buffer_load_lds(rx, (AS3 void*)(dst + k * 1024), 16, rel[k], xb, 0, AUX_X);
        }
    };
    auto issue_m = [&](int i) __attribute__((always_inline)) {
        const int mb = (win_h(i) * 64 + win_w(i)) * 8192 + wid * 2048;
#pragma unroll
        for (int k = 0; k < 2; ++k)
            __builtin_amdgcn_raw_ptr_buffer_load_lds(rm, (AS3 void*)(lds + L_M + wid * 2048 + k * 1024), 16, l16, mb + k * 1024, 0, AUX_M);
    };
    auto put_flag = [&](int i, u32x4 a, u32x4 c) __attribute__((always_inline)) {
        const unsigned o = (a[0] | a[1] | a[2]) | (a[3] | c[0] | c[1]) | (c[2] | c[3]);
        const bool nz = ANY(o != 0);
        if (lane == 0) *(AS3 unsigned*)(lds + L_FLAG + (i & 1) * 16 + wid * 4) = nz ? 1u : 0u;
    };

    ST(0);
    issue_x(0);
    u32x4 m0a, m0b;
    {
        const char* mb = mask + (size_t)(win_h(0) * 64 + win_w(0)) * 8192 + wid * 2048;
        m0a = __builtin_nontemporal_load((const u32x4*)(mb + l16));
        m0b = __builtin_nontemporal_load((const u32x4*)(mb + 1024 + l16));
    }
#pragma unroll
    for (int u = 0; u < 14; ++u) {
        const int q = wid + 4 * u;
        if (q < 54) __builtin_amdgcn_global_load_lds((const AS1 void*)(wqkv + q * 1024 + l16), (AS3 void*)(lds + L_STG + q * 1024), 16, 0, 0);
    }
    i32x4 id4[4];
#pragma unroll
    for (int u = 0; u < 4; ++u) id4[u] = idx[256 * u + tid];
    u32x2 rp = {0u, 0u};
    if (tid < 225) rp = rpb[tid];
    s16x4 wop[9];
    unsigned woa[9];
#pragma unroll
    for (int u = 0; u < 9; ++u) {
        const int id = tid + 256 * u, kq = id / 96, c = id - 96 * kq;
        const int d0 = 24 * (kq / 6) + 8 * (kq % 3) + 4 * ((kq % 6) / 3);
        const unsigned short* p = wo + d0 * 96 + c;
        wop[u] = (s16x4){(short)p[0], (short)p[96], (short)p[192], (short)p[288]};
        const int cl = c / 6, t6 = c - 6 * cl;
        woa[u] = L_WO + (t6 * 3 + (kq >> 3)) * 1024 + (16 * ((kq >> 1) & 3) + cl) * 16 + 8 * (kq & 1);
    }
#pragma unroll
    for (int u = 0; u < 4; ++u) {
        const int i = 16 * u + (tid >> 4), jq = tid & 15;
        int e[4];
#pragma unroll
        for (int k = 0; k < 4; ++k) { const int v = id4[u][k]; e[k] = (v < 0 ? v + 225 : v) * 4; }
        const u32x2 w2 = {(unsigned)e[0] | ((unsigned)e[1] << 16), (unsigned)e[2] | ((unsigned)e[3] << 16)};
        const int qt = i >> 5, kt = jq >> 3, g = (jq >> 1) & 3, hh = jq & 1;
        *(AS3 u32x2*)(lds + L_M + (((qt * 2 + kt) * 2 + (g >> 1)) * 64 + 32 * hh + (i & 31)) * 16 + (g & 1) * 8) = w2;
    }
    if (tid < 225) {
        *(AS3 float*)(lds + L_TAB + 0 * 900 + tid * 4) = U2F(rp[0] << 16) * LOG2E;
        *(AS3 float*)(lds + L_TAB + 1 * 900 + tid * 4) = U2F(rp[0] & 0xffff0000u) * LOG2E;
        *(AS3 float*)(lds + L_TAB + 2 * 900 + tid * 4) = U2F(rp[1] << 16) * LOG2E;
        *(AS3 float*)(lds + L_TAB + 3 * 900 + tid * 4) = U2F(rp[1] & 0xffff0000u) * LOG2E;
    }
    put_flag(0, m0a, m0b);
    ST(1);
    asm volatile("s_waitcnt vmcnt(0) lgkmcnt(0)\n\ts_barrier" ::: "memory");
    ST(2);

    s16x8 W[3][6];
    {
        AS3 char* wb = lds + L_STG + (8 * h + ((lane & 15) >> 2)) * 576 + (24 * wid + 16 * ((lane >> 4) & 1) + 4 * (lane & 3)) * 2;
#pragma unroll
        for (int P = 0; P < 3; ++P)
#pragma unroll
            for (int s = 0; s < 6; ++s) {
                const s16x4 lo = __builtin_amdgcn_ds_read_tr16_b64_v4i16((AS3 s16x4*)(wb + s * 9216 + P * 192));
                const s16x4 hi = __builtin_amdgcn_ds_read_tr16_b64_v4i16((AS3 s16x4*)(wb + s * 9216 + 2304 + P * 192));
                W[P][s] = __builtin_shufflevector(lo, hi, 0, 1, 2, 3, 4, 5, 6, 7);
            }
#pragma unroll
        for (int P = (ZPAD ? 0 : 2); P < 3; ++P)
#pragma unroll
        for (int s = 0; s < 6; ++s) {
            u32x4 v = __builtin_bit_cast(u32x4, W[P][s]);
#pragma unroll
            for (int k = 0; k < 4; ++k) v[k] = r < 24 ? v[k] : 0u;
            W[P][s] = as8(v);
        }
    }
    u32x4 ix[8];
#pragma unroll
    for (int u = 0; u < 8; ++u) ix[u] = *(AS3 u32x4*)(lds + L_M + u * 1024 + l16);
    asm volatile("s_waitcnt lgkmcnt(0)\n\ts_barrier" ::: "memory");
    SBAR();
    ST(3);
    issue_x(1);
    issue_m(1);
#pragma unroll
    for (int u = 0; u < 9; ++u) *(AS3 s16x4*)(lds + woa[u]) = wop[u];
    f32x16 bias[2][2];
    {
        AS3 char* tb = lds + L_TAB + wid * 900;
#pragma unroll
        for (int u = 0; u < 8; ++u)
#pragma unroll
            for (int k = 0; k < 4; ++k) {
                bias[u >> 2][(u >> 1) & 1][8 * (u & 1) + 2 * k] = *(AS3 float*)(tb + (ix[u][k] & 0xffffu));
                bias[u >> 2][(u >> 1) & 1][8 * (u & 1) + 2 * k + 1] = *(AS3 float*)(tb + (ix[u][k] >> 16));
            }
    }
    const unsigned ones = ((r & 27) == 24) ? 0x3f803f80u : 0u;
    const int gsw = (r >> 2) & 3;
    const unsigned xa0 = L_X + r * 192 + 16 * (h ^ gsw), xa1 = L_X + r * 192 + 16 * ((h ^ gsw) ^ 2);
    const unsigned owa = L_O + r * OSTR + 48 * wid + 24 * h;
    const unsigned ora = L_O + (16 * wid + 4 * (lane & 3) + ((lane & 15) >> 2)) * OSTR + 16 * (lane >> 4);
    const unsigned l12 = lane * 12;

    auto proj = [&](int i) __attribute__((always_inline)) {
        const unsigned oa = ora + (i & 1) * (64 * OSTR);
        s16x8 a[3];
#pragma unroll
        for (int s = 0; s < 3; ++s) a[s] = *(AS3 s16x8*)(lds + oa + 64 * s);
        f32x4 d[6];
#pragma unroll
        for (int t = 0; t < 6; ++t) {
            d[t] = (f32x4){0.f, 0.f, 0.f, 0.f};
#pragma unroll
            for (int s = 0; s < 3; ++s) d[t] = MFMA16(a[s], *(AS3 s16x8*)(lds + L_WO + (t * 3 + s) * 1024 + l16), d[t]);
        }
        const int wh = win_h(i), ww = win_w(i);
        const int ob = ((8 * wh + 4 + 2 * wid) & 511) * ROWB;
#pragma unroll
        for (int p = 0; p < 4; ++p) {
            const int o = ob + (p >> 1) * ROWB + ((8 * ww + 4 + 4 * (p & 1)) & 511) * 192;
            const u32x3 v = {pk(d[0][p], d[1][p]), pk(d[2][p], d[3][p]), pk(d[4][p], d[5][p])};
            __builtin_amdgcn_raw_buffer_store_b96(v, ro, l12, o, AUX_S);
        }
    };

    ST(4);
    for (int n = 0; n < NIT; ++n) {
        AC(5);
        if (n > 0) proj(n - 1);
        AC(0);
        __builtin_amdgcn_s_setprio(0);
        SBAR();
        u32x4 Q0[2], K0[2], V[2][2];
        u32x2 Q1[2], K1[2];
        const unsigned xo = (n & 1) * 12288;
#pragma unroll
        for (int tt = 0; tt < 2; ++tt) {
            s16x8 X[6];
#pragma unroll
            for (int s = 0; s < 6; ++s) X[s] = *(AS3 s16x8*)(lds + ((s & 1) ? xa1 : xa0) + xo + tt * 6144 + (s >> 1) * 64);
            f32x16 a = {}, c = {}, v = {};
#pragma unroll
            for (int s = 0; s < 6; ++s) a = MFMA32(W[0][s], X[s], a);
#pragma unroll
            for (int s = 0; s < 6; ++s) c = MFMA32(W[1][s], X[s], c);
#pragma unroll
            for (int s = 0; s < 6; ++s) v = MFMA32(X[s], W[2][s], v);
#pragma unroll
            for (int k = 0; k < 4; ++k) Q0[tt][k] = pk(a[2 * k] * QSCALE, a[2 * k + 1] * QSCALE);
#pragma unroll
            for (int k = 0; k < 2; ++k) Q1[tt][k] = pk(a[8 + 2 * k] * QSCALE, a[9 + 2 * k] * QSCALE);
#pragma unroll
            for (int k = 0; k < 4; ++k) K0[tt][k] = pk(c[2 * k], c[2 * k + 1]);
#pragma unroll
            for (int k = 0; k < 2; ++k) K1[tt][k] = pk(c[8 + 2 * k], c[9 + 2 * k]);
#pragma unroll
            for (int k = 0; k < 8; ++k) V[tt][k >> 2][k & 3] = pk(v[2 * k], v[2 * k + 1]) | ones;
            SBAR();
        }
        AC(1);
        __builtin_amdgcn_s_setprio(1);
        const u32x4 fl = *(AS3 u32x4*)(lds + L_FLAG + (n & 1) * 16);
        const bool masked = __builtin_amdgcn_readfirstlane(fl[0] | fl[1] | fl[2] | fl[3]) != 0;
        AS3 char* const ow = lds + owa + (n & 1) * (64 * OSTR);
        bool slow = masked;
        if (!masked) {
            bool bad = false;
#pragma unroll
            for (int qt = 0; qt < 2; ++qt) {
                f32x16 o = {};
#pragma unroll
                for (int kt = 0; kt < 2; ++kt) {
                    f32x16 s = MFMA32(as8(K0[kt]), as8(Q0[qt]), bias[qt][kt]);
                    s = MFMA32K8(as4(K1[kt]), as4(Q1[qt]), s);
#pragma unroll
                    for (int s2 = 0; s2 < 2; ++s2) {
                        u32x4 p;
#pragma unroll
                        for (int k = 0; k < 4; ++k) p[k] = pk(__builtin_amdgcn_exp2f(s[8 * s2 + 2 * k]), __builtin_amdgcn_exp2f(s[8 * s2 + 2 * k + 1]));
                        o = MFMA32(as8(V[kt][s2]), as8(p), o);
                    }
                }
                bad |= !(o[12] >= 0x1p-20f && o[12] <= 0x1p20f);
                put_o(ow + qt * (32 * OSTR), o);
            }
            slow = ANY(bad);
        }
        if (__builtin_expect(slow, 0)) {
            const int ms = (win_h(n) * 64 + win_w(n)) * 8192;
            const int l2 = lane_again();
            const int mv0 = (l2 & 31) * 128 + (l2 >> 5) * 8;
            put_o(ow, exact_tile(K0, K1, V, Q0[0], Q1[0], bias[0], rm, mv0, ms));
            SBAR();
            put_o(ow + 32 * OSTR, exact_tile(K0, K1, V, Q0[1], Q1[1], bias[1], rm, mv0, ms + 4096));
        }
        AC(2);
        if (n + 1 < NIT) {
            if (n == 0) asm volatile("s_waitcnt vmcnt(0)" ::: "memory"); else asm volatile("s_waitcnt vmcnt(4)" ::: "memory");
            AC(3);
            const u32x4 ma = *(AS3 u32x4*)(lds + L_M + wid * 2048 + l16), mb2 = *(AS3 u32x4*)(lds + L_M + wid * 2048 + 1024 + l16);
            put_flag(n + 1, ma, mb2);
        }
        asm volatile("s_waitcnt lgkmcnt(0)\n\ts_barrier" ::: "memory");
        AC(4);
        if (n + 2 < NIT) { issue_x(n + 2); issue_m(n + 2); }
        ST(5 + n);
    }
    proj(NIT - 1);
    ST(13);
}

template<int NT>
__global__ __launch_bounds__(256) void lin_k(const u32x4* __restrict__ x, const u32x4* __restrict__ m, u32x4* __restrict__ o){
  size_t b=blockIdx.x; int t=threadIdx.x;
  u32x4 a0,a1,a2,m0,m1;
  if(NT&1){ a0=__builtin_nontemporal_load(x+b*768+t); a1=__builtin_nontemporal_load(x+b*768+256+t); a2=__builtin_nontemporal_load(x+b*768+512+t); m0=__builtin_nontemporal_load(m+b*512+t); m1=__builtin_nontemporal_load(m+b*512+256+t);}
  else { a0=x[b*768+t]; a1=x[b*768+256+t]; a2=x[b*768+512+t]; m0=m[b*512+t]; m1=m[b*512+256+t]; }
  unsigned r=m0[0]|m0[1]|m0[2]|m0[3]|m1[0]|m1[1]|m1[2]|m1[3];
  a0[0]^=r;
  if(NT&2){ __builtin_nontemporal_store(a0,o+b*768+t); __builtin_nontemporal_store(a1,o+b*768+256+t); __builtin_nontemporal_store(a2,o+b*768+512+t);}
  else { o[b*768+t]=a0; o[b*768+256+t]=a1; o[b*768+512+t]=a2; }
}
extern "C" void kernel_launch(void* const* d_in, const int* in_sizes, int n_in, void* d_out, int out_size, void* d_ws, size_t ws_size, hipStream_t stream) {
    swin_kernel<<<NWG, 256, 0, stream>>>((const char*)d_in[0], (const char*)d_in[1], (const unsigned short*)d_in[2], (const u32x2*)d_in[3],
                                         (const i32x4*)d_in[4], (const char*)d_in[5], (char*)d_out
    );
}
```

```cpp
#include <hip/hip_runtime.h>
#include <hip/hip_bf16.h>

__global__ __launch_bounds__(512, 2) void swin_window_attn_kernel(const void* __restrict__ x, const void* __restrict__ wqkv, const void* __restrict__ wo,
                                                                 const void* __restrict__ rpb, const void* __restrict__ idx, const void* __restrict__ mask,
                                                                 void* __restrict__ out) {
    __shared__ __attribute__((aligned(16))) char lds[163648];
    const unsigned w8 = __builtin_amdgcn_readfirstlane(threadIdx.x >> 6);
    const unsigned wid = w8 & 3, bid = blockIdx.x + 256 * (w8 >> 2);
    const unsigned lbase = (unsigned)(size_t)(__attribute__((address_space(3))) char*)lds;
    asm volatile(
        "v_mbcnt_lo_u32_b32 v0, -1, 0\n"
        "v_mbcnt_hi_u32_b32 v0, -1, v0\n"
        "s_mov_b64 s[48:49], %[i]\n"
        "s_mov_b32 s50, 16384\n"
        "s_mov_b32 s51, 131072\n"
        "s_lshr_b32 s76, %[b], 8\n"
        "s_lshl2_add_u32 s70, s76, %[wid]\n"
        "s_lshl_b32 s96, s70, 10\n"
        "v_lshlrev_b32 v107, 4, v0\n"
        "v_lshlrev_b32 v1, 3, v0\n"
        "buffer_load_dwordx4 v[192:195], v107, s[48:51], s96 offen\n"
        "s_add_i32 s92, s96, 8192\n"
        "buffer_load_dwordx4 v[196:199], v107, s[48:51], s92 offen\n"
        "s_mov_b64 s[36:37], %[r]\n"
        "s_mov_b32 s38, 1800\n"
        "s_mov_b32 s39, 131072\n"
        "s_lshl_b32 s92, %[wid], 9\n"
        "buffer_load_dwordx2 v[208:209], v1, s[36:39], s92 offen\n"
        "s_mov_b64 s[52:53], %[w]\n"
        "s_mov_b32 s54, 55296\n"
        "s_mov_b32 s55, 131072\n"
        "s_bitcmp1_b32 224, s70\n"
        "s_cselect_b32 s58, -1, 0\n"
        "s_bitcmp1_b32 240, s70\n"
        "s_cselect_b32 s59, -1, 0\n"
        "s_bitcmp1_b32 193, s70\n"
        "s_cselect_b32 s60, -1, 0\n"
        "s_bitcmp1_b32 225, s70\n"
        "s_cselect_b32 s61, -1, 0\n"
        "v_xor_b32 v46, 32, v107\n"
        "v_cndmask_b32 v44, v107, v46, s[58:59]\n"
        "v_cndmask_b32 v45, v107, v46, s[60:61]\n"
        "s_add_i32 m0, s96, 18688\n"
        "s_add_i32 s97, s96, 0\n"
        "buffer_load_dwordx4 v44, s[52:55], s97 offen lds\n"
        "s_add_i32 m0, s96, 26880\n"
        "s_add_i32 s97, s96, 8192\n"
        "buffer_load_dwordx4 v45, s[52:55], s97 offen lds\n"
        "s_lshl_b32 s77, s76, 16\n"
        "s_mov_b32 s83, 98304\n"
        "s_mov_b32 s64, 0\n"
        "s_mov_b32 s33, 0\n"
        "s_mov_b64 s[28:29], %[x]\n"
        "s_mov_b32 s30, 50429952\n"
        "s_mov_b32 s31, 131072\n"
        "s_sub_u32 s28, s28, s83\n"
        "s_subb_u32 s29, s29, 0\n"
        "s_lshr_b32 s92, %[b], 6\n"
        "s_add_i32 s68, %[b], s92\n"
        "s_lshl_b32 s69, s92, 19\n"
        "s_lshl_b32 s67, s92, 3\n"
        "s_lshl_b32 s93, %[wid], 11\n"
        "s_add_i32 s69, s69, s93\n"
        "s_add_i32 s74, s93, 67840\n"
        "s_add_i32 s74, s74, s77\n"
        "s_lshl1_add_u32 s67, %[wid], s67\n"
        "s_addk_i32 s67, 4\n"
        "s_and_b32 s66, s68, 63\n"
        "s_cmp_eq_u32 s66, 63\n"
        "s_cselect_b32 s65, 32, 0\n"
        "s_cselect_b32 s66, 31, s66\n"
        "s_mov_b32 s85, s65\n"
        "s_mul_i32 s72, %[wid], 3072\n"
        "s_add_i32 s72, s72, s77\n"
        "s_lshl_b32 s73, s72, 1\n"
        "s_add_i32 s73, s73, 98816\n"
        "s_add_i32 s72, s72, 55552\n"
        "s_lshl3_add_u32 s56, s65, s67\n"
        "s_and_b32 s56, s56, 511\n"
        "s_mul_i32 s56, s56, s83\n"
        "s_mul_i32 s92, s66, 1536\n"
        "s_add_i32 s56, s56, s92\n"
        "s_addk_i32 s56, 768\n"
        "s_add_i32 s57, s56, 768\n"
        "s_mov_b32 s86, 0\n"
        "s_mov_b32 s87, 4294901760\n"
        "s_mov_b32 s88, -1\n"
        "s_mov_b32 s89, 0\n"
        "s_mov_b32 s90, 4294901760\n"
        "s_mov_b32 s91, -1\n"
        "s_mov_b32 s93, 195072\n"
        "v_cndmask_b32 v1, 0, 48, s[86:87]\n"
        "v_cndmask_b32 v2, 32, 48, s[88:89]\n"
        "v_cndmask_b32 v3, 32, 16, s[90:91]\n"
        "v_mov_b32 v4, s83\n"
        "v_mov_b32 v5, s93\n"
        "v_xor_b32 v1, v1, v107\n"
        "v_xor_b32 v2, v2, v107\n"
        "v_xor_b32 v3, v3, v107\n"
        "v_cndmask_b32 v6, v5, v4, s[88:89]\n"
        "v_add_u32 v78, v1, v4\n"
        "v_add_u32 v79, v2, v6\n"
        "v_add_u32 v80, v3, v5\n"
        "s_mov_b32 m0, s72\n"
        "s_nop 0\n"
        "buffer_load_dwordx4 v78, s[28:31], s56 offen lds\n"
        "buffer_load_dwordx4 v79, s[28:31], s56 offen offset:1024 lds\n"
        "buffer_load_dwordx4 v80, s[28:31], s56 offen offset:2048 lds\n"
        "s_sub_i32 s72, s73, s72\n"
        "s_mov_b64 s[60:61], s[56:57]\n"
        "s_mov_b32 s58, 1073741824\n"
        "s_mov_b32 s59, 1073741824\n"
        "s_mov_b32 s80, 0x3fb8aa3b\n"
        "v_and_b32 v1, 15, v0\n"
        "v_lshrrev_b32 v2, 4, v0\n"
        "v_lshrrev_b32 v3, 2, v1\n"
        "v_and_b32 v4, 3, v0\n"
        "s_lshl_b32 s92, %[wid], 2\n"
        "v_and_b32 v5, 7, v0\n"
        "v_add_u32 v6, s92, v2\n"
        "v_xor_b32 v5, v5, v6\n"
        "v_lshl_add_u32 v5, v4, 4, v5\n"
        "v_lshl_add_u32 v5, v3, 6, v5\n"
        "s_lshl_b32 s93, s76, 12\n"
        "s_add_i32 s93, s93, 67840\n"
        "v_lshl_add_u32 v48, v5, 4, s93\n"
        "s_lshl_b32 s92, %[wid], 6\n"
        "v_add_u32 v5, s92, v0\n"
        "v_lshlrev_b32 v49, 2, v5\n"
        "v_add_u32 v49, 160000, v49\n"
        "v_mov_b32 v6, 225\n"
        "v_cmp_lt_u32 s[24:25], v5, v6\n"
        "v_lshlrev_b32 v5, 4, v2\n"
        "v_xor_b32 v5, v5, v107\n"
        "v_add_u32 v50, 67840, v5\n"
        "v_xor_b32 v51, 64, v50\n"
        "s_mov_b32 s94, 160000\n"
        "v_mov_b32 v8, 0\n"
        "v_and_b32 v7, 7, v0\n"
        "v_lshlrev_b32 v7, 2, v7\n"
        "v_add_u32 v7, 163616, v7\n"
        "ds_write_b32 v7, v8\n"
        "s_waitcnt vmcnt(7)\n"
        "v_lshl_add_u32 v192, v192, 2, s94\n"
        "v_lshl_add_u32 v193, v193, 2, s94\n"
        "v_lshl_add_u32 v194, v194, 2, s94\n"
        "v_lshl_add_u32 v195, v195, 2, s94\n"
        "ds_write_b128 v48, v[192:195]\n"
        "s_waitcnt vmcnt(6)\n"
        "v_lshl_add_u32 v196, v196, 2, s94\n"
        "v_lshl_add_u32 v197, v197, 2, s94\n"
        "v_lshl_add_u32 v198, v198, 2, s94\n"
        "v_lshl_add_u32 v199, v199, 2, s94\n"
        "ds_write_b128 v48, v[196:199] offset:8192\n"
        "s_waitcnt vmcnt(5)\n"
        "v_lshlrev_b32 v60, 16, v208\n"
        "v_and_b32 v61, 4294901760, v208\n"
        "v_lshlrev_b32 v62, 16, v209\n"
        "v_and_b32 v63, 4294901760, v209\n"
        "v_mul_f32 v60, s80, v60\n"
        "v_mul_f32 v61, s80, v61\n"
        "v_mul_f32 v62, s80, v62\n"
        "v_mul_f32 v63, s80, v63\n"
        "s_mov_b64 exec, s[24:25]\n"
        "ds_write_b32 v49, v60\n"
        "ds_write_b32 v49, v61 offset:900\n"
        "ds_write_b32 v49, v62 offset:1800\n"
        "ds_write_b32 v49, v63 offset:2700\n"
        "s_mov_b64 exec, -1\n"
        "s_waitcnt lgkmcnt(0)\n"
        "s_barrier\n"
        "ds_read_b128 v[192:195], v50\n"
        "ds_read_b128 v[196:199], v51 offset:1024\n"
        "ds_read_b128 v[200:203], v50 offset:2048\n"
        "ds_read_b128 v[204:207], v51 offset:3072\n"
        "ds_read_b128 v[208:211], v50 offset:4096\n"
        "ds_read_b128 v[212:215], v51 offset:5120\n"
        "ds_read_b128 v[216:219], v50 offset:6144\n"
        "ds_read_b128 v[220:223], v51 offset:7168\n"
        "ds_read_b128 v[224:227], v50 offset:8192\n"
        "ds_read_b128 v[228:231], v51 offset:9216\n"
        "ds_read_b128 v[232:235], v50 offset:10240\n"
        "ds_read_b128 v[236:239], v51 offset:11264\n"
        "ds_read_b128 v[240:243], v50 offset:12288\n"
        "ds_read_b128 v[244:247], v51 offset:13312\n"
        "ds_read_b128 v[248:251], v50 offset:14336\n"
        "ds_read_b128 v[252:255], v51 offset:15360\n"
        "s_add_i32 m0, s96, 84224\n"
        "s_add_i32 s97, s96, 18432\n"
        "buffer_load_dwordx4 v44, s[52:55], s97 offen lds\n"
        "s_add_i32 m0, s96, 92416\n"
        "s_add_i32 s97, s96, 26624\n"
        "buffer_load_dwordx4 v45, s[52:55], s97 offen lds\n"
        "s_add_i32 m0, s96, 141568\n"
        "s_add_i32 s97, s96, 36864\n"
        "buffer_load_dwordx4 v44, s[52:55], s97 offen lds\n"
        "s_add_i32 m0, s96, 149760\n"
        "s_add_i32 s97, s96, 45056\n"
        "buffer_load_dwordx4 v45, s[52:55], s97 offen lds\n"
        "s_cmp_lt_u32 s70, 2\n"
        "s_cbranch_scc0 L_w3%=\n"
        "s_add_i32 m0, s96, 35072\n"
        "s_add_i32 s97, s96, 16384\n"
        "buffer_load_dwordx4 v46, s[52:55], s97 offen lds\n"
        "s_add_i32 m0, s96, 100608\n"
        "s_add_i32 s97, s96, 34816\n"
        "buffer_load_dwordx4 v46, s[52:55], s97 offen lds\n"
        "s_add_i32 m0, s96, 157952\n"
        "s_add_i32 s97, s96, 53248\n"
        "buffer_load_dwordx4 v46, s[52:55], s97 offen lds\n"
        "L_w3%=:\n"
        "s_mov_b64 s[48:49], %[wo]\n"
        "s_mov_b32 s50, 18432\n"
        "s_mov_b32 s51, 131072\n"
        "v_lshlrev_b32 v7, 2, v0\n"
        "s_lshl1_add_u32 s97, %[wid], s76\n"
        "s_mul_i32 s97, s97, 2304\n"
        "buffer_load_dword v88, v7, s[48:51], s97 offen\n"
        "buffer_load_dword v89, v7, s[48:51], s97 offen offset:192\n"
        "buffer_load_dword v90, v7, s[48:51], s97 offen offset:384\n"
        "buffer_load_dword v91, v7, s[48:51], s97 offen offset:576\n"
        "v_mul_u32_u24 v111, 12, v0\n"
        "v_sub_u32 v5, 0, v3\n"
        "v_and_b32 v5, 3, v5\n"
        "v_xor_b32 v5, v5, v2\n"
        "v_lshlrev_b32 v5, 4, v5\n"
        "v_mul_u32_u24 v6, 192, v1\n"
        "s_add_i32 s92, s77, 12288\n"
        "v_add3_u32 v103, v5, v6, s92\n"
        "v_lshl_add_u32 v5, v2, 3, v3\n"
        "v_mul_u32_u24 v5, 576, v5\n"
        "s_mul_i32 s92, %[wid], 48\n"
        "v_add_u32 v5, s92, v5\n"
        "v_lshl_add_u32 v52, v4, 3, v5\n"
        "v_lshrrev_b32 v6, 1, v4\n"
        "v_lshl_add_u32 v5, v6, 3, v5\n"
        "v_add_u32 v58, 32, v5\n"
        "v_and_b32 v6, 1, v2\n"
        "v_lshlrev_b32 v6, 5, v6\n"
        "v_xor_b32 v52, v52, v6\n"
        "v_xor_b32 v58, v58, v6\n"
        "v_and_b32 v6, 1, v4\n"
        "v_mul_u32_u24 v6, 192, v6\n"
        "v_add_u32 v54, v58, v6\n"
        "v_sub_u32 v56, v58, v6\n"
        "v_add_u32 v56, 192, v56\n"
        "v_add_u32 v53, 84224, v52\n"
        "v_add_u32 v55, 84224, v54\n"
        "v_add_u32 v57, 84224, v56\n"
        "v_add_u32 v59, 84224, v58\n"
        "buffer_load_dword v92, v7, s[48:51], s97 offen offset:768\n"
        "buffer_load_dword v93, v7, s[48:51], s97 offen offset:960\n"
        "buffer_load_dword v94, v7, s[48:51], s97 offen offset:1152\n"
        "buffer_load_dword v95, v7, s[48:51], s97 offen offset:1344\n"
        "v_lshlrev_b32 v5, 4, v1\n"
        "v_lshrrev_b32 v6, 1, v2\n"
        "v_lshl_add_u32 v6, v6, 10, v5\n"
        "v_and_b32 v8, 1, v2\n"
        "v_lshl_add_u32 v6, v8, 3, v6\n"
        "s_lshl_b32 s92, %[wid], 11\n"
        "s_add_i32 s92, s92, s77\n"
        "v_add_u32 v81, s92, v6\n"
        "s_lshl_b32 s92, %[wid], 10\n"
        "s_add_i32 s92, s92, s77\n"
        "s_addk_i32 s92, 8192\n"
        "v_lshl_add_u32 v6, v2, 2, v5\n"
        "v_add_u32 v82, s92, v6\n"
        "buffer_load_dword v96, v7, s[48:51], s97 offen offset:1536\n"
        "buffer_load_dword v97, v7, s[48:51], s97 offen offset:1728\n"
        "buffer_load_dword v98, v7, s[48:51], s97 offen offset:1920\n"
        "buffer_load_dword v99, v7, s[48:51], s97 offen offset:2112\n"
        "v_lshl_add_u32 v5, v4, 2, v3\n"
        "v_lshlrev_b32 v5, 4, v5\n"
        "v_lshl_add_u32 v5, v2, 10, v5\n"
        "s_lshl_b32 s92, %[wid], 8\n"
        "s_add_i32 s92, s92, s77\n"
        "v_add_u32 v115, s92, v5\n"
        "v_mov_b32 v119, 0\n"
        "v_mov_b32 v123, 0\n"
        "v_mov_b32 v127, 0\n"
        "v_mov_b32 v131, 0\n"
        "s_cmp_lg_u32 %[wid], 0\n"
        "s_cbranch_scc1 L_g1_1%=\n"
        "s_waitcnt lgkmcnt(12)\n"
        "ds_read_b32 v192, v192\n"
        "ds_read_b32 v193, v193\n"
        "ds_read_b32 v194, v194\n"
        "ds_read_b32 v195, v195\n"
        "ds_read_b32 v196, v196\n"
        "ds_read_b32 v197, v197\n"
        "ds_read_b32 v198, v198\n"
        "ds_read_b32 v199, v199\n"
        "ds_read_b32 v200, v200\n"
        "ds_read_b32 v201, v201\n"
        "ds_read_b32 v202, v202\n"
        "ds_read_b32 v203, v203\n"
        "ds_read_b32 v204, v204\n"
        "ds_read_b32 v205, v205\n"
        "ds_read_b32 v206, v206\n"
        "ds_read_b32 v207, v207\n"
        "s_waitcnt lgkmcnt(14)\n"
        "ds_read_b32 v208, v208\n"
        "ds_read_b32 v209, v209\n"
        "ds_read_b32 v210, v210\n"
        "ds_read_b32 v211, v211\n"
        "ds_read_b32 v212, v212\n"
        "ds_read_b32 v213, v213\n"
        "ds_read_b32 v214, v214\n"
        "ds_read_b32 v215, v215\n"
        "ds_read_b32 v216, v216\n"
        "ds_read_b32 v217, v217\n"
        "ds_read_b32 v218, v218\n"
        "ds_read_b32 v219, v219\n"
        "ds_read_b32 v220, v220\n"
        "ds_read_b32 v221, v221\n"
        "ds_read_b32 v222, v222\n"
        "ds_read_b32 v223, v223\n"
        "ds_read_b32 v224, v224\n"
        "ds_read_b32 v225, v225\n"
        "ds_read_b32 v226, v226\n"
        "ds_read_b32 v227, v227\n"
        "ds_read_b32 v228, v228\n"
        "ds_read_b32 v229, v229\n"
        "ds_read_b32 v230, v230\n"
        "ds_read_b32 v231, v231\n"
        "ds_read_b32 v232, v232\n"
        "ds_read_b32 v233, v233\n"
        "ds_read_b32 v234, v234\n"
        "ds_read_b32 v235, v235\n"
        "ds_read_b32 v236, v236\n"
        "ds_read_b32 v237, v237\n"
        "ds_read_b32 v238, v238\n"
        "ds_read_b32 v239, v239\n"
        "ds_read_b32 v240, v240\n"
        "ds_read_b32 v241, v241\n"
        "ds_read_b32 v242, v242\n"
        "ds_read_b32 v243, v243\n"
        "ds_read_b32 v244, v244\n"
        "ds_read_b32 v245, v245\n"
        "ds_read_b32 v246, v246\n"
        "ds_read_b32 v247, v247\n"
        "ds_read_b32 v248, v248\n"
        "ds_read_b32 v249, v249\n"
        "ds_read_b32 v250, v250\n"
        "ds_read_b32 v251, v251\n"
        "ds_read_b32 v252, v252\n"
        "ds_read_b32 v253, v253\n"
        "ds_read_b32 v254, v254\n"
        "ds_read_b32 v255, v255\n"
        "s_branch L_g1_d%=\n"
        "L_g1_1%=:\n"
        "s_cmp_lg_u32 %[wid], 1\n"
        "s_cbranch_scc1 L_g1_2%=\n"
        "s_waitcnt lgkmcnt(12)\n"
        "ds_read_b32 v192, v192 offset:900\n"
        "ds_read_b32 v193, v193 offset:900\n"
        "ds_read_b32 v194, v194 offset:900\n"
        "ds_read_b32 v195, v195 offset:900\n"
        "ds_read_b32 v196, v196 offset:900\n"
        "ds_read_b32 v197, v197 offset:900\n"
        "ds_read_b32 v198, v198 offset:900\n"
        "ds_read_b32 v199, v199 offset:900\n"
        "ds_read_b32 v200, v200 offset:900\n"
        "ds_read_b32 v201, v201 offset:900\n"
        "ds_read_b32 v202, v202 offset:900\n"
        "ds_read_b32 v203, v203 offset:900\n"
        "ds_read_b32 v204, v204 offset:900\n"
        "ds_read_b32 v205, v205 offset:900\n"
        "ds_read_b32 v206, v206 offset:900\n"
        "ds_read_b32 v207, v207 offset:900\n"
        "s_waitcnt lgkmcnt(14)\n"
        "ds_read_b32 v208, v208 offset:900\n"
        "ds_read_b32 v209, v209 offset:900\n"
        "ds_read_b32 v210, v210 offset:900\n"
        "ds_read_b32 v211, v211 offset:900\n"
        "ds_read_b32 v212, v212 offset:900\n"
        "ds_read_b32 v213, v213 offset:900\n"
        "ds_read_b32 v214, v214 offset:900\n"
        "ds_read_b32 v215, v215 offset:900\n"
        "ds_read_b32 v216, v216 offset:900\n"
        "ds_read_b32 v217, v217 offset:900\n"
        "ds_read_b32 v218, v218 offset:900\n"
        "ds_read_b32 v219, v219 offset:900\n"
        "ds_read_b32 v220, v220 offset:900\n"
        "ds_read_b32 v221, v221 offset:900\n"
        "ds_read_b32 v222, v222 offset:900\n"
        "ds_read_b32 v223, v223 offset:900\n"
        "ds_read_b32 v224, v224 offset:900\n"
        "ds_read_b32 v225, v225 offset:900\n"
        "ds_read_b32 v226, v226 offset:900\n"
        "ds_read_b32 v227, v227 offset:900\n"
        "ds_read_b32 v228, v228 offset:900\n"
        "ds_read_b32 v229, v229 offset:900\n"
        "ds_read_b32 v230, v230 offset:900\n"
        "ds_read_b32 v231, v231 offset:900\n"
        "ds_read_b32 v232, v232 offset:900\n"
        "ds_read_b32 v233, v233 offset:900\n"
        "ds_read_b32 v234, v234 offset:900\n"
        "ds_read_b32 v235, v235 offset:900\n"
        "ds_read_b32 v236, v236 offset:900\n"
        "ds_read_b32 v237, v237 offset:900\n"
        "ds_read_b32 v238, v238 offset:900\n"
        "ds_read_b32 v239, v239 offset:900\n"
        "ds_read_b32 v240, v240 offset:900\n"
        "ds_read_b32 v241, v241 offset:900\n"
        "ds_read_b32 v242, v242 offset:900\n"
        "ds_read_b32 v243, v243 offset:900\n"
        "ds_read_b32 v244, v244 offset:900\n"
        "ds_read_b32 v245, v245 offset:900\n"
        "ds_read_b32 v246, v246 offset:900\n"
        "ds_read_b32 v247, v247 offset:900\n"
        "ds_read_b32 v248, v248 offset:900\n"
        "ds_read_b32 v249, v249 offset:900\n"
        "ds_read_b32 v250, v250 offset:900\n"
        "ds_read_b32 v251, v251 offset:900\n"
        "ds_read_b32 v252, v252 offset:900\n"
        "ds_read_b32 v253, v253 offset:900\n"
        "ds_read_b32 v254, v254 offset:900\n"
        "ds_read_b32 v255, v255 offset:900\n"
        "s_branch L_g1_d%=\n"
        "L_g1_2%=:\n"
        "s_cmp_lg_u32 %[wid], 2\n"
        "s_cbranch_scc1 L_g1_3%=\n"
        "s_waitcnt lgkmcnt(12)\n"
        "ds_read_b32 v192, v192 offset:1800\n"
        "ds_read_b32 v193, v193 offset:1800\n"
        "ds_read_b32 v194, v194 offset:1800\n"
        "ds_read_b32 v195, v195 offset:1800\n"
        "ds_read_b32 v196, v196 offset:1800\n"
        "ds_read_b32 v197, v197 offset:1800\n"
        "ds_read_b32 v198, v198 offset:1800\n"
        "ds_read_b32 v199, v199 offset:1800\n"
        "ds_read_b32 v200, v200 offset:1800\n"
        "ds_read_b32 v201, v201 offset:1800\n"
        "ds_read_b32 v202, v202 offset:1800\n"
        "ds_read_b32 v203, v203 offset:1800\n"
        "ds_read_b32 v204, v204 offset:1800\n"
        "ds_read_b32 v205, v205 offset:1800\n"
        "ds_read_b32 v206, v206 offset:1800\n"
        "ds_read_b32 v207, v207 offset:1800\n"
        "s_waitcnt lgkmcnt(14)\n"
        "ds_read_b32 v208, v208 offset:1800\n"
        "ds_read_b32 v209, v209 offset:1800\n"
        "ds_read_b32 v210, v210 offset:1800\n"
        "ds_read_b32 v211, v211 offset:1800\n"
        "ds_read_b32 v212, v212 offset:1800\n"
        "ds_read_b32 v213, v213 offset:1800\n"
        "ds_read_b32 v214, v214 offset:1800\n"
        "ds_read_b32 v215, v215 offset:1800\n"
        "ds_read_b32 v216, v216 offset:1800\n"
        "ds_read_b32 v217, v217 offset:1800\n"
        "ds_read_b32 v218, v218 offset:1800\n"
        "ds_read_b32 v219, v219 offset:1800\n"
        "ds_read_b32 v220, v220 offset:1800\n"
        "ds_read_b32 v221, v221 offset:1800\n"
        "ds_read_b32 v222, v222 offset:1800\n"
        "ds_read_b32 v223, v223 offset:1800\n"
        "ds_read_b32 v224, v224 offset:1800\n"
        "ds_read_b32 v225, v225 offset:1800\n"
        "ds_read_b32 v226, v226 offset:1800\n"
        "ds_read_b32 v227, v227 offset:1800\n"
        "ds_read_b32 v228, v228 offset:1800\n"
        "ds_read_b32 v229, v229 offset:1800\n"
        "ds_read_b32 v230, v230 offset:1800\n"
        "ds_read_b32 v231, v231 offset:1800\n"
        "ds_read_b32 v232, v232 offset:1800\n"
        "ds_read_b32 v233, v233 offset:1800\n"
        "ds_read_b32 v234, v234 offset:1800\n"
        "ds_read_b32 v235, v235 offset:1800\n"
        "ds_read_b32 v236, v236 offset:1800\n"
        "ds_read_b32 v237, v237 offset:1800\n"
        "ds_read_b32 v238, v238 offset:1800\n"
        "ds_read_b32 v239, v239 offset:1800\n"
        "ds_read_b32 v240, v240 offset:1800\n"
        "ds_read_b32 v241, v241 offset:1800\n"
        "ds_read_b32 v242, v242 offset:1800\n"
        "ds_read_b32 v243, v243 offset:1800\n"
        "ds_read_b32 v244, v244 offset:1800\n"
        "ds_read_b32 v245, v245 offset:1800\n"
        "ds_read_b32 v246, v246 offset:1800\n"
        "ds_read_b32 v247, v247 offset:1800\n"
        "ds_read_b32 v248, v248 offset:1800\n"
        "ds_read_b32 v249, v249 offset:1800\n"
        "ds_read_b32 v250, v250 offset:1800\n"
        "ds_read_b32 v251, v251 offset:1800\n"
        "ds_read_b32 v252, v252 offset:1800\n"
        "ds_read_b32 v253, v253 offset:1800\n"
        "ds_read_b32 v254, v254 offset:1800\n"
        "ds_read_b32 v255, v255 offset:1800\n"
        "s_branch L_g1_d%=\n"
        "L_g1_3%=:\n"
        "s_waitcnt lgkmcnt(12)\n"
        "ds_read_b32 v192, v192 offset:2700\n"
        "ds_read_b32 v193, v193 offset:2700\n"
        "ds_read_b32 v194, v194 offset:2700\n"
        "ds_read_b32 v195, v195 offset:2700\n"
        "ds_read_b32 v196, v196 offset:2700\n"
        "ds_read_b32 v197, v197 offset:2700\n"
        "ds_read_b32 v198, v198 offset:2700\n"
        "ds_read_b32 v199, v199 offset:2700\n"
        "ds_read_b32 v200, v200 offset:2700\n"
        "ds_read_b32 v201, v201 offset:2700\n"
        "ds_read_b32 v202, v202 offset:2700\n"
        "ds_read_b32 v203, v203 offset:2700\n"
        "ds_read_b32 v204, v204 offset:2700\n"
        "ds_read_b32 v205, v205 offset:2700\n"
        "ds_read_b32 v206, v206 offset:2700\n"
        "ds_read_b32 v207, v207 offset:2700\n"
        "s_waitcnt lgkmcnt(14)\n"
        "ds_read_b32 v208, v208 offset:2700\n"
        "ds_read_b32 v209, v209 offset:2700\n"
        "ds_read_b32 v210, v210 offset:2700\n"
        "ds_read_b32 v211, v211 offset:2700\n"
        "ds_read_b32 v212, v212 offset:2700\n"
        "ds_read_b32 v213, v213 offset:2700\n"
        "ds_read_b32 v214, v214 offset:2700\n"
        "ds_read_b32 v215, v215 offset:2700\n"
        "ds_read_b32 v216, v216 offset:2700\n"
        "ds_read_b32 v217, v217 offset:2700\n"
        "ds_read_b32 v218, v218 offset:2700\n"
        "ds_read_b32 v219, v219 offset:2700\n"
        "ds_read_b32 v220, v220 offset:2700\n"
        "ds_read_b32 v221, v221 offset:2700\n"
        "ds_read_b32 v222, v222 offset:2700\n"
        "ds_read_b32 v223, v223 offset:2700\n"
        "ds_read_b32 v224, v224 offset:2700\n"
        "ds_read_b32 v225, v225 offset:2700\n"
        "ds_read_b32 v226, v226 offset:2700\n"
        "ds_read_b32 v227, v227 offset:2700\n"
        "ds_read_b32 v228, v228 offset:2700\n"
        "ds_read_b32 v229, v229 offset:2700\n"
        "ds_read_b32 v230, v230 offset:2700\n"
        "ds_read_b32 v231, v231 offset:2700\n"
        "ds_read_b32 v232, v232 offset:2700\n"
        "ds_read_b32 v233, v233 offset:2700\n"
        "ds_read_b32 v234, v234 offset:2700\n"
        "ds_read_b32 v235, v235 offset:2700\n"
        "ds_read_b32 v236, v236 offset:2700\n"
        "ds_read_b32 v237, v237 offset:2700\n"
        "ds_read_b32 v238, v238 offset:2700\n"
        "ds_read_b32 v239, v239 offset:2700\n"
        "ds_read_b32 v240, v240 offset:2700\n"
        "ds_read_b32 v241, v241 offset:2700\n"
        "ds_read_b32 v242, v242 offset:2700\n"
        "ds_read_b32 v243, v243 offset:2700\n"
        "ds_read_b32 v244, v244 offset:2700\n"
        "ds_read_b32 v245, v245 offset:2700\n"
        "ds_read_b32 v246, v246 offset:2700\n"
        "ds_read_b32 v247, v247 offset:2700\n"
        "ds_read_b32 v248, v248 offset:2700\n"
        "ds_read_b32 v249, v249 offset:2700\n"
        "ds_read_b32 v250, v250 offset:2700\n"
        "ds_read_b32 v251, v251 offset:2700\n"
        "ds_read_b32 v252, v252 offset:2700\n"
        "ds_read_b32 v253, v253 offset:2700\n"
        "ds_read_b32 v254, v254 offset:2700\n"
        "ds_read_b32 v255, v255 offset:2700\n"
        "L_g1_d%=:\n"
        "s_mov_b64 s[36:37], %[m]\n"
        "s_mov_b32 s38, 33554432\n"
        "s_mov_b32 s39, 131072\n"
        "s_lshl_b32 s44, s65, 19\n"
        "s_lshl_b32 s93, s66, 13\n"
        "s_add_i32 s44, s44, s93\n"
        "s_add_i32 s44, s44, s69\n"
        "s_add_i32 s65, s65, 8\n"
        "s_and_b32 s65, s65, 63\n"
        "s_add_i32 s66, s68, s65\n"
        "s_and_b32 s66, s66, 63\n"
        "s_lshl3_add_u32 s56, s65, s67\n"
        "s_and_b32 s56, s56, 511\n"
        "s_mul_i32 s56, s56, s83\n"
        "s_mul_i32 s92, s66, 1536\n"
        "s_add_i32 s56, s56, s92\n"
        "s_addk_i32 s56, 768\n"
        "s_cmp_eq_u32 s66, 63\n"
        "s_cselect_b32 s71, s83, 0\n"
        "s_sub_i32 s57, s56, s71\n"
        "s_addk_i32 s57, 768\n"
        "s_mov_b64 s[62:63], s[56:57]\n"
        "s_lshl_b32 s45, s65, 19\n"
        "s_lshl_b32 s93, s66, 13\n"
        "s_add_i32 s45, s45, s93\n"
        "s_add_i32 s45, s45, s69\n"
        "s_add_i32 s65, s65, 8\n"
        "s_and_b32 s65, s65, 63\n"
        "s_add_i32 s66, s68, s65\n"
        "s_and_b32 s66, s66, 63\n"
        "s_sub_i32 s94, s71, s33\n"
        "s_mov_b32 s33, s71\n"
        "s_mov_b64 exec, s[86:87]\n"
        "v_subrev_u32 v78, s94, v78\n"
        "s_mov_b64 exec, s[88:89]\n"
        "v_subrev_u32 v79, s94, v79\n"
        "s_mov_b64 exec, s[90:91]\n"
        "v_subrev_u32 v80, s94, v80\n"
        "s_mov_b64 exec, -1\n"
        "s_mov_b64 s[40:41], %[o]\n"
        "s_mov_b32 s42, 50331648\n"
        "s_mov_b32 s43, 131072\n"
        "s_mov_b32 s78, 0x3e96c740\n"
        "s_mov_b32 s79, 0x49800000\n"
        "s_mov_b32 s75, 12288\n"
        "s_add_i32 s81, s70, 163624\n"
        "s_mov_b32 s98, 33686018\n"
        "s_mov_b32 s82, 163624\n"
        "s_mov_b32 s26, 858993459\n"
        "s_mov_b32 s27, 858993459\n"
        "s_lshr_b32 s52, %[wid], 1\n"
        "s_lshl_b32 s52, s52, 10\n"
        "s_and_b32 s53, %[wid], 1\n"
        "s_lshl_b32 s53, s53, 9\n"
        "s_add_i32 s52, s52, s53\n"
        "s_lshl_b32 s53, %[wid], 8\n"
        "s_addk_i32 s53, 2040\n"
        "s_add_i32 s54, s52, 240\n"
        "s_mul_i32 s94, s76, 264\n"
        "s_cmp_lg_u32 s76, 0\n"
        "s_cselect_b32 s54, s53, s54\n"
        "s_cselect_b32 s53, s53, s52\n"
        "s_add_i32 s52, s52, s94\n"
        "v_mul_u32_u24 v5, 86, v0\n"
        "v_lshrrev_b32 v5, 8, v5\n"
        "v_mad_i32_i24 v6, v5, -3, v0\n"
        "v_mul_u32_u24 v6, 6240, v6\n"
        "v_lshl_add_u32 v5, v5, 4, v6\n"
        "v_add_u32 v84, s52, v5\n"
        "v_add_u32 v85, s53, v5\n"
        "v_add_u32 v86, s54, v5\n"
        "s_mov_b32 s52, 84148480\n"
        "s_mov_b32 s53, 117834498\n"
        "s_mov_b32 s84, 0\n"
        "s_mov_b32 s24, 1145324612\n"
        "s_mov_b32 s25, 1145324612\n"
        "s_waitcnt vmcnt(0) lgkmcnt(0)\n"
        "s_barrier\n"
        "ds_read_b64_tr_b16 v[132:133], v52 offset:18688\n"
        "ds_read_b64_tr_b16 v[134:135], v52 offset:20992\n"
        "ds_read_b64_tr_b16 v[136:137], v53\n"
        "ds_read_b64_tr_b16 v[138:139], v53 offset:2304\n"
        "ds_read_b64_tr_b16 v[140:141], v53 offset:57344\n"
        "ds_read_b64_tr_b16 v[142:143], v53 offset:59648\n"
        "ds_read_b128 v[0:3], v103 offset:43264\n"
        "ds_read_b128 v[4:7], v103 offset:43328\n"
        "ds_read_b128 v[8:11], v103 offset:43392\n"
        "s_mov_b32 m0, s72\n"
        "s_nop 0\n"
        "buffer_load_dwordx4 v78, s[28:31], s56 offen lds\n"
        "ds_read_b64_tr_b16 v[156:157], v52 offset:18880\n"
        "ds_read_b64_tr_b16 v[158:159], v52 offset:21184\n"
        "ds_read_b64_tr_b16 v[160:161], v53 offset:192\n"
        "ds_read_b64_tr_b16 v[162:163], v53 offset:2496\n"
        "ds_read_b64_tr_b16 v[164:165], v53 offset:57536\n"
        "ds_read_b64_tr_b16 v[166:167], v53 offset:59840\n"
        "buffer_load_dwordx4 v79, s[28:31], s56 offen offset:1024 lds\n"
        "ds_read_b128 v[40:43], v103 offset:46336\n"
        "ds_read_b128 v[44:47], v103 offset:46400\n"
        "ds_read_b128 v[48:51], v103 offset:46464\n"
        "ds_read_b64_tr_b16 v[144:145], v54 offset:18688\n"
        "ds_read_b64_tr_b16 v[146:147], v54 offset:20992\n"
        "ds_read_b64_tr_b16 v[148:149], v55\n"
        "ds_read_b64_tr_b16 v[150:151], v55 offset:2304\n"
        "ds_read_b64_tr_b16 v[152:153], v55 offset:57344\n"
        "ds_read_b64_tr_b16 v[154:155], v55 offset:59648\n"
        "buffer_load_dwordx4 v80, s[28:31], s56 offen offset:2048 lds\n"
        "s_sub_i32 s72, s73, s72\n"
        "ds_read_b64_tr_b16 v[28:29], v56 offset:18688\n"
        "ds_read_b64_tr_b16 v[30:31], v56 offset:20992\n"
        "ds_read_b64_tr_b16 v[32:33], v57\n"
        "ds_read_b64_tr_b16 v[34:35], v57 offset:2304\n"
        "ds_read_b64_tr_b16 v[36:37], v57 offset:57344\n"
        "ds_read_b64_tr_b16 v[38:39], v57 offset:59648\n"
        "buffer_load_dwordx4 v[60:63], v107, s[36:39], s44 offen\n"
        "buffer_load_dwordx4 v[64:67], v107, s[36:39], s44 offen offset:1024\n"
        "ds_read_b64_tr_b16 v[168:169], v52 offset:19072\n"
        "ds_read_b64_tr_b16 v[170:171], v52 offset:21376\n"
        "ds_read_b64_tr_b16 v[172:173], v53 offset:384\n"
        "ds_read_b64_tr_b16 v[174:175], v53 offset:2688\n"
        "ds_read_b64_tr_b16 v[176:177], v53 offset:57728\n"
        "ds_read_b64_tr_b16 v[178:179], v53 offset:60032\n"
        "ds_read_b64_tr_b16 v[180:181], v58 offset:19072\n"
        "ds_read_b64_tr_b16 v[182:183], v58 offset:21376\n"
        "ds_read_b64_tr_b16 v[184:185], v59 offset:384\n"
        "ds_read_b64_tr_b16 v[186:187], v59 offset:2688\n"
        "ds_read_b64_tr_b16 v[188:189], v59 offset:57728\n"
        "ds_read_b64_tr_b16 v[190:191], v59 offset:60032\n"
        "buffer_load_dwordx4 v[68:71], v107, s[36:39], s45 offen\n"
        "buffer_load_dwordx4 v[72:75], v107, s[36:39], s45 offen offset:1024\n"
        "s_waitcnt lgkmcnt(14)\n"
        "v_mfma_f32_16x16x32_bf16 v[16:19], v[132:135], v[0:3], 0\n"
        "s_setprio 3\n"
        "v_cndmask_b32 v12, 0, 1.0, s[24:25]\n"
        "v_cndmask_b32 v13, 0, 1.0, s[24:25]\n"
        "v_mfma_f32_16x16x32_bf16 v[16:19], v[136:139], v[4:7], v[16:19]\n"
        "s_mov_b32 vcc_lo, 3284386755\n"
        "s_mov_b32 vcc_hi, 3284386755\n"
        "v_cndmask_b32 v14, 0, 1.0, s[24:25]\n"
        "v_cndmask_b32 v15, 0, 1.0, s[24:25]\n"
        "v_mfma_f32_16x16x32_bf16 v[16:19], v[140:143], v[8:11], v[16:19]\n"
        "v_mfma_f32_16x16x32_bf16 v[20:23], v[156:159], v[0:3], 0\n"
        "v_mfma_f32_16x16x32_bf16 v[20:23], v[160:163], v[4:7], v[20:23]\n"
        "v_mfma_f32_16x16x32_bf16 v[20:23], v[164:167], v[8:11], v[20:23]\n"
        "s_waitcnt lgkmcnt(12)\n"
        "v_cndmask_b32_dpp v144, v28, v144, vcc quad_perm:[2,3,0,1] row_mask:0xf bank_mask:0xf\n"
        "v_cndmask_b32_dpp v145, v29, v145, vcc quad_perm:[2,3,0,1] row_mask:0xf bank_mask:0xf\n"
        "v_cndmask_b32_dpp v146, v30, v146, vcc quad_perm:[2,3,0,1] row_mask:0xf bank_mask:0xf\n"
        "v_cndmask_b32_dpp v147, v31, v147, vcc quad_perm:[2,3,0,1] row_mask:0xf bank_mask:0xf\n"
        "v_cndmask_b32_dpp v148, v32, v148, vcc quad_perm:[2,3,0,1] row_mask:0xf bank_mask:0xf\n"
        "v_cndmask_b32_dpp v149, v33, v149, vcc quad_perm:[2,3,0,1] row_mask:0xf bank_mask:0xf\n"
        "v_cndmask_b32_dpp v150, v34, v150, vcc quad_perm:[2,3,0,1] row_mask:0xf bank_mask:0xf\n"
        "v_cndmask_b32_dpp v151, v35, v151, vcc quad_perm:[2,3,0,1] row_mask:0xf bank_mask:0xf\n"
        "v_cndmask_b32_dpp v152, v36, v152, vcc quad_perm:[2,3,0,1] row_mask:0xf bank_mask:0xf\n"
        "v_cndmask_b32_dpp v153, v37, v153, vcc quad_perm:[2,3,0,1] row_mask:0xf bank_mask:0xf\n"
        "v_cndmask_b32_dpp v154, v38, v154, vcc quad_perm:[2,3,0,1] row_mask:0xf bank_mask:0xf\n"
        "v_cndmask_b32_dpp v155, v39, v155, vcc quad_perm:[2,3,0,1] row_mask:0xf bank_mask:0xf\n"
        "v_mfma_f32_16x16x32_bf16 v[24:27], v[132:135], v[40:43], 0\n"
        "v_mfma_f32_16x16x32_bf16 v[24:27], v[136:139], v[44:47], v[24:27]\n"
        "v_mfma_f32_16x16x32_bf16 v[24:27], v[140:143], v[48:51], v[24:27]\n"
        "v_perm_b32 v52, v89, v88, s52\n"
        "v_perm_b32 v53, v91, v90, s52\n"
        "v_perm_b32 v54, v89, v88, s53\n"
        "v_perm_b32 v55, v91, v90, s53\n"
        "v_mfma_f32_16x16x32_bf16 v[28:31], v[156:159], v[40:43], 0\n"
        "s_mov_b32 exec_hi, 65535\n"
        "ds_write_b64 v84, v[52:53]\n"
        "ds_write_b64 v84, v[54:55] offset:3120\n"
        "s_mov_b32 exec_hi, -1\n"
        "v_mfma_f32_16x16x32_bf16 v[28:31], v[160:163], v[44:47], v[28:31]\n"
        "v_mul_f32 v16, s78, v16\n"
        "v_mul_f32 v17, s78, v17\n"
        "v_mfma_f32_16x16x32_bf16 v[28:31], v[164:167], v[48:51], v[28:31]\n"
        "s_waitcnt lgkmcnt(2)\n"
        "v_mul_f32 v18, s78, v18\n"
        "v_mul_f32 v19, s78, v19\n"
        "v_perm_b32 v56, v93, v92, s52\n"
        "v_perm_b32 v57, v95, v94, s52\n"
        "v_perm_b32 v58, v93, v92, s53\n"
        "v_perm_b32 v59, v95, v94, s53\n"
        "v_mov_b32_dpp v180, v180 quad_perm:[2,3,2,3] row_mask:0xf bank_mask:0xa\n"
        "v_mov_b32_dpp v181, v181 quad_perm:[2,3,2,3] row_mask:0xf bank_mask:0xa\n"
        "v_mov_b32_dpp v182, v182 quad_perm:[2,3,2,3] row_mask:0xf bank_mask:0xa\n"
        "v_mov_b32_dpp v183, v183 quad_perm:[2,3,2,3] row_mask:0xf bank_mask:0xa\n"
        "v_mov_b32_dpp v184, v184 quad_perm:[2,3,2,3] row_mask:0xf bank_mask:0xa\n"
        "v_mov_b32_dpp v185, v185 quad_perm:[2,3,2,3] row_mask:0xf bank_mask:0xa\n"
        "v_mov_b32_dpp v186, v186 quad_perm:[2,3,2,3] row_mask:0xf bank_mask:0xa\n"
        "v_mov_b32_dpp v187, v187 quad_perm:[2,3,2,3] row_mask:0xf bank_mask:0xa\n"
        "v_mov_b32_dpp v188, v188 quad_perm:[2,3,2,3] row_mask:0xf bank_mask:0xa\n"
        "v_mov_b32_dpp v189, v189 quad_perm:[2,3,2,3] row_mask:0xf bank_mask:0xa\n"
        "v_mov_b32_dpp v190, v190 quad_perm:[2,3,2,3] row_mask:0xf bank_mask:0xa\n"
        "v_mov_b32_dpp v191, v191 quad_perm:[2,3,2,3] row_mask:0xf bank_mask:0xa\n"
        "v_mfma_f32_16x16x32_bf16 v[32:35], v[144:147], v[0:3], 0\n"
        "s_mov_b32 exec_hi, 65535\n"
        "ds_write_b64 v85, v[56:57] offset:8\n"
        "ds_write_b64 v85, v[58:59] offset:3128\n"
        "s_mov_b32 exec_hi, -1\n"
        "v_cvt_pk_bf16_f32 v116, v16, v17\n"
        "v_cvt_pk_bf16_f32 v117, v18, v19\n"
        "v_mfma_f32_16x16x32_bf16 v[32:35], v[148:151], v[4:7], v[32:35]\n"
        "v_cvt_pk_bf16_f32 v100, v20, v21\n"
        "v_cvt_pk_bf16_f32 v101, v22, v23\n"
        "v_mfma_f32_16x16x32_bf16 v[32:35], v[152:155], v[8:11], v[32:35]\n"
        "v_mul_f32 v24, s78, v24\n"
        "v_mul_f32 v25, s78, v25\n"
        "v_perm_b32 v52, v97, v96, s52\n"
        "v_perm_b32 v53, v99, v98, s52\n"
        "v_perm_b32 v54, v97, v96, s53\n"
        "v_perm_b32 v55, v99, v98, s53\n"
        "v_cndmask_b32 v180, 0, v180, s[26:27]\n"
        "v_cndmask_b32 v181, 0, v181, s[26:27]\n"
        "v_cndmask_b32 v182, 0, v182, s[26:27]\n"
        "v_cndmask_b32 v183, 0, v183, s[26:27]\n"
        "v_cndmask_b32 v184, 0, v184, s[26:27]\n"
        "v_cndmask_b32 v185, 0, v185, s[26:27]\n"
        "v_cndmask_b32 v186, 0, v186, s[26:27]\n"
        "v_cndmask_b32 v187, 0, v187, s[26:27]\n"
        "v_cndmask_b32 v188, 0, v188, s[26:27]\n"
        "v_cndmask_b32 v189, 0, v189, s[26:27]\n"
        "v_cndmask_b32 v190, 0, v190, s[26:27]\n"
        "v_cndmask_b32 v191, 0, v191, s[26:27]\n"
        "v_mfma_f32_16x16x32_bf16 v[36:39], v[144:147], v[40:43], 0\n"
        "s_mov_b32 exec_hi, 65535\n"
        "ds_write_b64 v86, v[52:53] offset:16\n"
        "ds_write_b64 v86, v[54:55] offset:3136\n"
        "s_mov_b32 exec_hi, -1\n"
        "v_mul_f32 v26, s78, v26\n"
        "v_mul_f32 v27, s78, v27\n"
        "v_mfma_f32_16x16x32_bf16 v[36:39], v[148:151], v[44:47], v[36:39]\n"
        "v_cvt_pk_bf16_f32 v120, v24, v25\n"
        "v_cvt_pk_bf16_f32 v121, v26, v27\n"
        "v_mfma_f32_16x16x32_bf16 v[36:39], v[152:155], v[48:51], v[36:39]\n"
        "v_cvt_pk_bf16_f32 v104, v28, v29\n"
        "v_cvt_pk_bf16_f32 v105, v30, v31\n"
        "v_mfma_f32_16x16x32_bf16 v[16:19], v[0:3], v[168:171], 0\n"
        "s_mul_i32 s92, s66, 1536\n"
        "v_cvt_pk_bf16_f32 v102, v34, v35\n"
        "v_mul_f32 v32, s78, v32\n"
        "v_mfma_f32_16x16x32_bf16 v[16:19], v[4:7], v[172:175], v[16:19]\n"
        "s_add_u32 s44, s40, s83\n"
        "s_addc_u32 s45, s41, 0\n"
        "s_lshl3_add_u32 s56, s65, s67\n"
        "s_and_b32 s56, s56, 511\n"
        "s_mul_i32 s56, s56, s83\n"
        "s_add_i32 s56, s56, s92\n"
        "s_addk_i32 s56, 768\n"
        "s_cmp_eq_u32 s66, 63\n"
        "s_cselect_b32 s71, s83, 0\n"
        "s_sub_i32 s57, s56, s71\n"
        "s_addk_i32 s57, 768\n"
        "s_cmp_lt_u32 s64, 6\n"
        "s_cselect_b32 s56, s56, 1073741824\n"
        "s_lshl_b32 s70, s65, 19\n"
        "s_lshl_b32 s93, s66, 13\n"
        "s_add_i32 s70, s70, s93\n"
        "s_add_i32 s70, s70, s69\n"
        "s_cmp_lt_u32 s64, 6\n"
        "s_cselect_b32 s70, s70, 1073741824\n"
        "s_add_i32 s65, s65, 8\n"
        "s_and_b32 s65, s65, 63\n"
        "s_add_i32 s66, s68, s65\n"
        "s_and_b32 s66, s66, 63\n"
        "v_mul_f32 v33, s78, v33\n"
        "v_cvt_pk_bf16_f32 v118, v32, v33\n"
        "v_mfma_f32_16x16x32_bf16 v[16:19], v[8:11], v[176:179], v[16:19]\n"
        "s_mov_b32 s46, 50233344\n"
        "v_cvt_pk_bf16_f32 v106, v38, v39\n"
        "v_mul_f32 v36, s78, v36\n"
        "s_sub_i32 s94, s71, s33\n"
        "s_mov_b32 s33, s71\n"
        "s_mov_b64 exec, s[86:87]\n"
        "v_subrev_u32 v78, s94, v78\n"
        "s_mov_b64 exec, s[88:89]\n"
        "v_subrev_u32 v79, s94, v79\n"
        "s_mov_b64 exec, s[90:91]\n"
        "v_subrev_u32 v80, s94, v80\n"
        "s_mov_b64 exec, -1\n"
        "v_mfma_f32_16x16x32_bf16 v[20:23], v[0:3], v[180:183], v[12:15]\n"
        "ds_read_b128 v[0:3], v103 offset:49408\n"
        "v_mul_f32 v37, s78, v37\n"
        "v_cvt_pk_bf16_f32 v122, v36, v37\n"
        "v_mfma_f32_16x16x32_bf16 v[20:23], v[4:7], v[184:187], v[20:23]\n"
        "s_mov_b32 s47, 131072\n"
        "ds_read_b128 v[4:7], v103 offset:49472\n"
        "v_mfma_f32_16x16x32_bf16 v[20:23], v[8:11], v[188:191], v[20:23]\n"
        "ds_read_b128 v[8:11], v103 offset:49536\n"
        "s_waitcnt lgkmcnt(0)\n"
        "s_barrier\n"
        "v_mfma_f32_16x16x32_bf16 v[24:27], v[40:43], v[168:171], 0\n"
        "v_cvt_pk_bf16_f32 v84, v16, v17\n"
        "v_cvt_pk_bf16_f32 v85, v18, v19\n"
        "v_mfma_f32_16x16x32_bf16 v[24:27], v[44:47], v[172:175], v[24:27]\n"
        "v_mfma_f32_16x16x32_bf16 v[24:27], v[48:51], v[176:179], v[24:27]\n"
        "v_mfma_f32_16x16x32_bf16 v[28:31], v[40:43], v[180:183], v[12:15]\n"
        "ds_read_b128 v[40:43], v103 offset:52480\n"
        "v_mfma_f32_16x16x32_bf16 v[28:31], v[44:47], v[184:187], v[28:31]\n"
        "ds_read_b128 v[44:47], v103 offset:52544\n"
        "v_mfma_f32_16x16x32_bf16 v[28:31], v[48:51], v[188:191], v[28:31]\n"
        "ds_read_b128 v[48:51], v103 offset:52608\n"
        "v_cvt_pk_bf16_f32 v92, v20, v21\n"
        "v_cvt_pk_bf16_f32 v93, v22, v23\n"
        "v_mfma_f32_16x16x32_bf16 v[32:35], v[132:135], v[0:3], 0\n"
        "s_waitcnt vmcnt(1)\n"
        "v_or3_b32 v68, v68, v69, v70\n"
        "s_waitcnt vmcnt(0)\n"
        "v_or3_b32 v72, v72, v73, v74\n"
        "v_mfma_f32_16x16x32_bf16 v[32:35], v[136:139], v[4:7], v[32:35]\n"
        "v_or3_b32 v68, v68, v71, v75\n"
        "v_or_b32 v68, v68, v72\n"
        "v_mfma_f32_16x16x32_bf16 v[32:35], v[140:143], v[8:11], v[32:35]\n"
        "v_cmp_ne_u32 vcc, 0, v68\n"
        "v_or3_b32 v60, v60, v61, v62\n"
        "v_mfma_f32_16x16x32_bf16 v[36:39], v[144:147], v[0:3], 0\n"
        "s_nop 1\n"
        "s_cmp_lg_u64 vcc, 0\n"
        "s_cselect_b32 s95, 1, 0\n"
        "s_or_b32 s55, s95, s98\n"
        "v_cvt_pk_bf16_f32 v86, v24, v25\n"
        "v_cvt_pk_bf16_f32 v87, v26, v27\n"
        "v_mfma_f32_16x16x32_bf16 v[36:39], v[148:151], v[4:7], v[36:39]\n"
        "v_or3_b32 v64, v64, v65, v66\n"
        "v_or3_b32 v60, v60, v63, v67\n"
        "v_mfma_f32_16x16x32_bf16 v[36:39], v[152:155], v[8:11], v[36:39]\n"
        "s_mov_b32 m0, s74\n"
        "v_or_b32 v60, v60, v64\n"
        "v_cmp_ne_u32 vcc, 0, v60\n"
        "v_mfma_f32_16x16x32_bf16 v[16:19], v[156:159], v[0:3], 0\n"
        "s_nop 2\n"
        "s_cmp_lg_u64 vcc, 0\n"
        "s_cselect_b32 s95, 128, 0\n"
        "s_or_b32 s55, s95, s55\n"
        "v_cvt_pk_bf16_f32 v94, v28, v29\n"
        "v_cvt_pk_bf16_f32 v95, v30, v31\n"
        "v_mfma_f32_16x16x32_bf16 v[16:19], v[160:163], v[4:7], v[16:19]\n"
        "buffer_load_dwordx4 v107, s[36:39], s70 offen lds\n"
        "v_mov_b32 v57, s81\n"
        "v_mov_b32 v56, s55\n"
        "v_mfma_f32_16x16x32_bf16 v[16:19], v[164:167], v[8:11], v[16:19]\n"
        "ds_write_b8 v57, v56\n"
        "v_xor_b32 v103, 12288, v103\n"
        "v_mul_f32 v32, s78, v32\n"
        "v_mfma_f32_16x16x32_bf16 v[20:23], v[0:3], v[168:171], 0\n"
        "buffer_load_dwordx4 v107, s[36:39], s70 offen offset:1024 lds\n"
        "v_mul_f32 v33, s78, v33\n"
        "v_mul_f32 v34, s78, v34\n"
        "v_mfma_f32_16x16x32_bf16 v[20:23], v[4:7], v[172:175], v[20:23]\n"
        "v_mul_f32 v35, s78, v35\n"
        "v_cvt_pk_bf16_f32 v124, v32, v33\n"
        "v_mfma_f32_16x16x32_bf16 v[20:23], v[8:11], v[176:179], v[20:23]\n"
        "v_cvt_pk_bf16_f32 v125, v34, v35\n"
        "v_cvt_pk_bf16_f32 v110, v38, v39\n"
        "v_mfma_f32_16x16x32_bf16 v[24:27], v[0:3], v[180:183], v[12:15]\n"
        "v_mul_f32 v36, s78, v36\n"
        "v_mul_f32 v37, s78, v37\n"
        "v_mfma_f32_16x16x32_bf16 v[24:27], v[4:7], v[184:187], v[24:27]\n"
        "v_cvt_pk_bf16_f32 v126, v36, v37\n"
        "v_cvt_pk_bf16_f32 v108, v16, v17\n"
        "v_mfma_f32_16x16x32_bf16 v[24:27], v[8:11], v[188:191], v[24:27]\n"
        "s_waitcnt lgkmcnt(1)\n"
        "v_cvt_pk_bf16_f32 v109, v18, v19\n"
        "v_cvt_pk_bf16_f32 v88, v20, v21\n"
        "v_mov_b32 v58, s82\n"
        "v_mfma_f32_16x16x32_bf16 v[28:31], v[156:159], v[40:43], 0\n"
        "ds_read_b64 v[58:59], v58\n"
        "v_cvt_pk_bf16_f32 v89, v22, v23\n"
        "v_mfma_f32_16x16x32_bf16 v[28:31], v[160:163], v[44:47], v[28:31]\n"
        "v_mfma_f32_16x16x32_bf16 v[28:31], v[164:167], v[48:51], v[28:31]\n"
        "v_mfma_f32_16x16x32_bf16 v[32:35], v[144:147], v[40:43], 0\n"
        "v_mfma_f32_16x16x32_bf16 v[32:35], v[148:151], v[44:47], v[32:35]\n"
        "v_cvt_pk_bf16_f32 v96, v24, v25\n"
        "v_cvt_pk_bf16_f32 v97, v26, v27\n"
        "v_mfma_f32_16x16x32_bf16 v[32:35], v[152:155], v[48:51], v[32:35]\n"
        "s_waitcnt lgkmcnt(0)\n"
        "v_readfirstlane_b32 s77, v58\n"
        "v_readfirstlane_b32 s99, v59\n"
        "s_bitcmp1_b32 %[b], 8\n"
        "s_nop 2\n"
        "s_cselect_b32 s94, s99, s77\n"
        "s_cselect_b32 s99, s77, s99\n"
        "s_mov_b32 s77, s94\n"
        "s_and_b32 s94, s77, 2122219134\n"
        "s_cmp_lg_u32 s94, s98\n"
        "s_cbranch_scc1 L_sp3%=\n"
        "L_sp3b%=:\n"
        "s_mov_b32 m0, s72\n"
        "v_mfma_f32_16x16x32_bf16 v[36:39], v[40:43], v[168:171], 0\n"
        "buffer_load_dwordx4 v78, s[28:31], s56 offen lds\n"
        "v_mfma_f32_16x16x32_bf16 v[36:39], v[44:47], v[172:175], v[36:39]\n"
        "buffer_load_dwordx4 v79, s[28:31], s56 offen offset:1024 lds\n"
        "v_mfma_f32_16x16x32_bf16 v[36:39], v[48:51], v[176:179], v[36:39]\n"
        "buffer_load_dwordx4 v80, s[28:31], s56 offen offset:2048 lds\n"
        "v_cvt_pk_bf16_f32 v112, v28, v29\n"
        "v_cvt_pk_bf16_f32 v113, v30, v31\n"
        "v_mfma_f32_16x16x32_bf16 v[16:19], v[40:43], v[180:183], v[12:15]\n"
        "s_sub_i32 s72, s73, s72\n"
        "s_and_b32 s26, s77, 2155905152\n"
        "s_mov_b32 s48, 0\n"
        "v_mfma_f32_16x16x32_bf16 v[16:19], v[44:47], v[184:187], v[16:19]\n"
        "v_mfma_f32_16x16x32_bf16 v[16:19], v[48:51], v[188:191], v[16:19]\n"
        "v_cvt_pk_bf16_f32 v114, v34, v35\n"
        "v_mul_f32 v32, s78, v32\n"
        "v_mul_f32 v33, s78, v33\n"
        "v_cvt_pk_bf16_f32 v130, v32, v33\n"
        "v_mfma_f32_16x16x32_bf16 v[32:35], v[132:135], v[40:43], 0\n"
        "v_cvt_pk_bf16_f32 v90, v36, v37\n"
        "v_cvt_pk_bf16_f32 v91, v38, v39\n"
        "v_mfma_f32_16x16x32_bf16 v[0:3], v[100:103], v[116:119], v[192:195]\n"
        "v_cvt_pk_bf16_f32 v98, v16, v17\n"
        "v_cvt_pk_bf16_f32 v99, v18, v19\n"
        "v_mfma_f32_16x16x32_bf16 v[32:35], v[136:139], v[44:47], v[32:35]\n"
        "s_setprio 2\n"
        "v_mfma_f32_16x16x32_bf16 v[4:7], v[104:107], v[116:119], v[196:199]\n"
        "v_mfma_f32_16x16x32_bf16 v[32:35], v[140:143], v[48:51], v[32:35]\n"
        "v_mfma_f32_16x16x32_bf16 v[8:11], v[108:111], v[116:119], v[200:203]\n"
        "v_mfma_f32_16x16x32_bf16 v[12:15], v[112:115], v[116:119], v[204:207]\n"
        "v_exp_f32 v0, v0\n"
        "v_exp_f32 v1, v1\n"
        "v_exp_f32 v2, v2\n"
        "v_exp_f32 v3, v3\n"
        "v_mfma_f32_16x16x32_bf16 v[16:19], v[100:103], v[120:123], v[208:211]\n"
        "v_exp_f32 v4, v4\n"
        "v_exp_f32 v5, v5\n"
        "v_mfma_f32_16x16x32_bf16 v[20:23], v[104:107], v[120:123], v[212:215]\n"
        "v_exp_f32 v6, v6\n"
        "v_exp_f32 v7, v7\n"
        "v_cvt_pk_bf16_f32 v0, v0, v1\n"
        "v_cvt_pk_bf16_f32 v1, v2, v3\n"
        "v_cvt_pk_bf16_f32 v2, v4, v5\n"
        "v_cvt_pk_bf16_f32 v3, v6, v7\n"
        "v_mul_f32 v32, s78, v32\n"
        "v_mul_f32 v33, s78, v33\n"
        "v_mul_f32 v34, s78, v34\n"
        "v_mul_f32 v35, s78, v35\n"
        "v_cvt_pk_bf16_f32 v128, v32, v33\n"
        "v_cvt_pk_bf16_f32 v129, v34, v35\n"
        "v_mfma_f32_16x16x32_bf16 v[24:27], v[108:111], v[120:123], v[216:219]\n"
        "s_xor_b32 s81, s81, 8\n"
        "s_xor_b32 s82, s82, 8\n"
        "s_mov_b64 s[34:35], s[58:59]\n"
        "v_mfma_f32_16x16x32_bf16 v[28:31], v[112:115], v[120:123], v[220:223]\n"
        "s_and_b32 s76, s77, 16843009\n"
        "s_add_i32 s98, s98, 33686018\n"
        "v_exp_f32 v8, v8\n"
        "v_mfma_f32_16x16x32_bf16 v[32:35], v[84:87], v[0:3], 0\n"
        "s_mov_b64 s[58:59], s[60:61]\n"
        "v_exp_f32 v9, v9\n"
        "v_mfma_f32_16x16x32_bf16 v[36:39], v[92:95], v[0:3], 0\n"
        "s_mov_b64 s[60:61], s[62:63]\n"
        "v_exp_f32 v10, v10\n"
        "v_exp_f32 v11, v11\n"
        "v_exp_f32 v12, v12\n"
        "v_exp_f32 v13, v13\n"
        "v_exp_f32 v14, v14\n"
        "v_exp_f32 v15, v15\n"
        "v_cvt_pk_bf16_f32 v8, v8, v9\n"
        "v_cvt_pk_bf16_f32 v9, v10, v11\n"
        "v_cvt_pk_bf16_f32 v10, v12, v13\n"
        "v_cvt_pk_bf16_f32 v11, v14, v15\n"
        "v_exp_f32 v16, v16\n"
        "v_exp_f32 v17, v17\n"
        "v_mfma_f32_16x16x32_bf16 v[32:35], v[88:91], v[8:11], v[32:35]\n"
        "s_mov_b64 s[62:63], s[56:57]\n"
        "v_exp_f32 v18, v18\n"
        "v_mfma_f32_16x16x32_bf16 v[36:39], v[96:99], v[8:11], v[36:39]\n"
        "v_exp_f32 v19, v19\n"
        "v_mfma_f32_16x16x32_bf16 v[0:3], v[100:103], v[124:127], v[224:227]\n"
        "v_exp_f32 v20, v20\n"
        "v_exp_f32 v21, v21\n"
        "v_mfma_f32_16x16x32_bf16 v[4:7], v[104:107], v[124:127], v[228:231]\n"
        "v_exp_f32 v22, v22\n"
        "v_exp_f32 v23, v23\n"
        "s_nop 0\n"
        "v_rcp_f32 v39, v38\n"
        "v_cvt_pk_bf16_f32 v16, v16, v17\n"
        "v_cvt_pk_bf16_f32 v17, v18, v19\n"
        "v_cvt_pk_bf16_f32 v18, v20, v21\n"
        "v_cvt_pk_bf16_f32 v19, v22, v23\n"
        "v_mul_f32 v32, v39, v32\n"
        "v_mul_f32 v33, v39, v33\n"
        "v_mul_f32 v34, v39, v34\n"
        "v_mul_f32 v35, v39, v35\n"
        "v_cvt_pk_bf16_f32 v32, v32, v33\n"
        "v_cvt_pk_bf16_f32 v33, v34, v35\n"
        "v_mfma_f32_16x16x32_bf16 v[8:11], v[108:111], v[124:127], v[232:235]\n"
        "ds_write_b64 v81, v[32:33] offset:18688\n"
        "v_mul_f32 v36, v39, v36\n"
        "v_mul_f32 v37, v39, v37\n"
        "v_cvt_pk_bf16_f32 v36, v36, v37\n"
        "v_max_f32 v76, v38, v39\n"
        "v_mfma_f32_16x16x32_bf16 v[12:15], v[112:115], v[124:127], v[236:239]\n"
        "ds_write_b32 v82, v36 offset:18688\n"
        "v_exp_f32 v24, v24\n"
        "v_mfma_f32_16x16x32_bf16 v[32:35], v[84:87], v[16:19], 0\n"
        "v_exp_f32 v25, v25\n"
        "v_mfma_f32_16x16x32_bf16 v[36:39], v[92:95], v[16:19], 0\n"
        "v_exp_f32 v26, v26\n"
        "v_exp_f32 v27, v27\n"
        "v_exp_f32 v28, v28\n"
        "v_exp_f32 v29, v29\n"
        "v_exp_f32 v30, v30\n"
        "v_exp_f32 v31, v31\n"
        "v_cvt_pk_bf16_f32 v24, v24, v25\n"
        "v_cvt_pk_bf16_f32 v25, v26, v27\n"
        "v_cvt_pk_bf16_f32 v26, v28, v29\n"
        "v_cvt_pk_bf16_f32 v27, v30, v31\n"
        "v_exp_f32 v0, v0\n"
        "v_exp_f32 v1, v1\n"
        "v_mfma_f32_16x16x32_bf16 v[32:35], v[88:91], v[24:27], v[32:35]\n"
        "v_exp_f32 v2, v2\n"
        "v_mfma_f32_16x16x32_bf16 v[36:39], v[96:99], v[24:27], v[36:39]\n"
        "v_exp_f32 v3, v3\n"
        "v_mfma_f32_16x16x32_bf16 v[16:19], v[100:103], v[128:131], v[240:243]\n"
        "v_exp_f32 v4, v4\n"
        "v_exp_f32 v5, v5\n"
        "v_mfma_f32_16x16x32_bf16 v[20:23], v[104:107], v[128:131], v[244:247]\n"
        "v_exp_f32 v6, v6\n"
        "v_exp_f32 v7, v7\n"
        "s_nop 0\n"
        "v_rcp_f32 v39, v38\n"
        "v_cvt_pk_bf16_f32 v0, v0, v1\n"
        "v_cvt_pk_bf16_f32 v1, v2, v3\n"
        "v_cvt_pk_bf16_f32 v2, v4, v5\n"
        "v_cvt_pk_bf16_f32 v3, v6, v7\n"
        "v_mul_f32 v32, v39, v32\n"
        "v_mul_f32 v33, v39, v33\n"
        "v_mul_f32 v34, v39, v34\n"
        "v_mul_f32 v35, v39, v35\n"
        "v_cvt_pk_bf16_f32 v32, v32, v33\n"
        "v_cvt_pk_bf16_f32 v33, v34, v35\n"
        "v_mfma_f32_16x16x32_bf16 v[24:27], v[108:111], v[128:131], v[248:251]\n"
        "ds_write_b64 v81, v[32:33] offset:18944\n"
        "v_mul_f32 v36, v39, v36\n"
        "v_mul_f32 v37, v39, v37\n"
        "v_cvt_pk_bf16_f32 v36, v36, v37\n"
        "v_max3_f32 v76, v76, v38, v39\n"
        "v_mfma_f32_16x16x32_bf16 v[28:31], v[112:115], v[128:131], v[252:255]\n"
        "ds_write_b32 v82, v36 offset:18944\n"
        "v_exp_f32 v8, v8\n"
        "v_mfma_f32_16x16x32_bf16 v[32:35], v[84:87], v[0:3], 0\n"
        "v_exp_f32 v9, v9\n"
        "v_mfma_f32_16x16x32_bf16 v[36:39], v[92:95], v[0:3], 0\n"
        "v_exp_f32 v10, v10\n"
        "v_exp_f32 v11, v11\n"
        "v_exp_f32 v12, v12\n"
        "v_exp_f32 v13, v13\n"
        "v_exp_f32 v14, v14\n"
        "v_exp_f32 v15, v15\n"
        "v_cvt_pk_bf16_f32 v8, v8, v9\n"
        "v_cvt_pk_bf16_f32 v9, v10, v11\n"
        "v_cvt_pk_bf16_f32 v10, v12, v13\n"
        "v_cvt_pk_bf16_f32 v11, v14, v15\n"
        "v_exp_f32 v16, v16\n"
        "v_exp_f32 v17, v17\n"
        "v_mfma_f32_16x16x32_bf16 v[32:35], v[88:91], v[8:11], v[32:35]\n"
        "ds_read_b128 v[0:3], v103 offset:43264\n"
        "v_exp_f32 v18, v18\n"
        "v_mfma_f32_16x16x32_bf16 v[36:39], v[96:99], v[8:11], v[36:39]\n"
        "ds_read_b128 v[4:7], v103 offset:43328\n"
        "v_exp_f32 v19, v19\n"
        "v_exp_f32 v20, v20\n"
        "v_exp_f32 v21, v21\n"
        "v_exp_f32 v22, v22\n"
        "v_exp_f32 v23, v23\n"
        "v_cvt_pk_bf16_f32 v16, v16, v17\n"
        "v_cvt_pk_bf16_f32 v17, v18, v19\n"
        "v_rcp_f32 v39, v38\n"
        "v_cvt_pk_bf16_f32 v18, v20, v21\n"
        "v_cvt_pk_bf16_f32 v19, v22, v23\n"
        "v_mul_f32 v32, v39, v32\n"
        "v_mul_f32 v33, v39, v33\n"
        "v_mul_f32 v34, v39, v34\n"
        "v_mul_f32 v35, v39, v35\n"
        "v_cvt_pk_bf16_f32 v32, v32, v33\n"
        "v_cvt_pk_bf16_f32 v33, v34, v35\n"
        "ds_write_b64 v81, v[32:33] offset:19200\n"
        "v_mul_f32 v36, v39, v36\n"
        "v_mul_f32 v37, v39, v37\n"
        "v_cvt_pk_bf16_f32 v36, v36, v37\n"
        "v_max3_f32 v76, v76, v38, v39\n"
        "v_exp_f32 v24, v24\n"
        "v_mfma_f32_16x16x32_bf16 v[32:35], v[84:87], v[16:19], 0\n"
        "ds_read_b128 v[8:11], v103 offset:43392\n"
        "ds_write_b32 v82, v36 offset:19200\n"
        "v_exp_f32 v25, v25\n"
        "v_mfma_f32_16x16x32_bf16 v[36:39], v[92:95], v[16:19], 0\n"
        "v_exp_f32 v26, v26\n"
        "v_exp_f32 v27, v27\n"
        "v_exp_f32 v28, v28\n"
        "v_exp_f32 v29, v29\n"
        "v_exp_f32 v30, v30\n"
        "v_exp_f32 v31, v31\n"
        "v_cvt_pk_bf16_f32 v24, v24, v25\n"
        "v_cvt_pk_bf16_f32 v25, v26, v27\n"
        "v_cvt_pk_bf16_f32 v26, v28, v29\n"
        "v_cvt_pk_bf16_f32 v27, v30, v31\n"
        "s_nop 1\n"
        "v_mfma_f32_16x16x32_bf16 v[32:35], v[88:91], v[24:27], v[32:35]\n"
        "s_add_i32 s64, s64, 1\n"
        "s_cmp_lt_u32 s64, 8\n"
        "v_mfma_f32_16x16x32_bf16 v[36:39], v[96:99], v[24:27], v[36:39]\n"
        "s_waitcnt lgkmcnt(0)\n"
        "s_cmp_lg_u32 s26, 0\n"
        "s_cbranch_scc1 L_redo%=\n"
        "L_top%=:\n"
        "s_cmp_lg_u32 s76, 0\n"
        "s_cbranch_scc1 L_masked%=\n"
        "v_mfma_f32_16x16x32_bf16 v[16:19], v[156:159], v[0:3], 0\n"
        "s_setprio 3\n"
        "s_cmp_eq_u32 s84, 0\n"
        "s_cbranch_scc1 L_p16%=\n"
        "s_setprio 1\n"
        "L_p16%=:\n"
        "v_cvt_pk_bf16_f32 v118, v44, v48\n"
        "v_cvt_pk_bf16_f32 v122, v45, v49\n"
        "v_mfma_f32_16x16x32_bf16 v[16:19], v[160:163], v[4:7], v[16:19]\n"
        "ds_read_b128 v[40:43], v103 offset:46336\n"
        "v_cvt_pk_bf16_f32 v126, v46, v50\n"
        "v_rcp_f32 v39, v38\n"
        "v_mfma_f32_16x16x32_bf16 v[16:19], v[164:167], v[8:11], v[16:19]\n"
        "v_cvt_pk_bf16_f32 v130, v47, v51\n"
        "v_max3_f32 v76, v76, v38, v39\n"
        "v_mfma_f32_16x16x32_bf16 v[20:23], v[0:3], v[168:171], 0\n"
        "ds_read_b128 v[44:47], v103 offset:46400\n"
        "v_cmp_lt_f32 vcc, s79, v76\n"
        "v_cndmask_b32 v12, 0, 1.0, s[24:25]\n"
        "v_mfma_f32_16x16x32_bf16 v[20:23], v[4:7], v[172:175], v[20:23]\n"
        "ds_read_b128 v[48:51], v103 offset:46464\n"
        "v_cndmask_b32 v13, 0, 1.0, s[24:25]\n"
        "v_cndmask_b32 v14, 0, 1.0, s[24:25]\n"
        "v_mfma_f32_16x16x32_bf16 v[20:23], v[8:11], v[176:179], v[20:23]\n"
        "s_cbranch_vccnz L_redo%=\n"
        "v_cndmask_b32 v15, 0, 1.0, s[24:25]\n"
        "v_mul_f32 v32, v39, v32\n"
        "s_nop 0\n"
        "v_mfma_f32_16x16x32_bf16 v[24:27], v[0:3], v[180:183], v[12:15]\n"
        "v_mul_f32 v33, v39, v33\n"
        "v_mul_f32 v34, v39, v34\n"
        "v_mfma_f32_16x16x32_bf16 v[24:27], v[4:7], v[184:187], v[24:27]\n"
        "buffer_store_dwordx3 v[116:118], v111, s[40:43], s34 offen sc1\n"
        "v_mul_f32 v35, v39, v35\n"
        "v_cvt_pk_bf16_f32 v32, v32, v33\n"
        "v_mfma_f32_16x16x32_bf16 v[24:27], v[8:11], v[188:191], v[24:27]\n"
        "s_lshl3_add_u32 s56, s65, s67\n"
        "v_cvt_pk_bf16_f32 v33, v34, v35\n"
        "v_mul_f32 v36, v39, v36\n"
        "v_mfma_f32_16x16x32_bf16 v[28:31], v[144:147], v[0:3], 0\n"
        "ds_write_b64 v81, v[32:33] offset:19456\n"
        "v_cvt_pk_bf16_f32 v100, v16, v17\n"
        "v_cvt_pk_bf16_f32 v101, v18, v19\n"
        "v_mfma_f32_16x16x32_bf16 v[28:31], v[148:151], v[4:7], v[28:31]\n"
        "s_and_b32 s56, s56, 511\n"
        "v_mul_f32 v37, v39, v37\n"
        "v_cvt_pk_bf16_f32 v36, v36, v37\n"
        "v_mfma_f32_16x16x32_bf16 v[28:31], v[152:155], v[8:11], v[28:31]\n"
        "ds_write_b32 v82, v36 offset:19456\n"
        "v_add_u32 v81, s75, v81\n"
        "v_add_u32 v82, s75, v82\n"
        "v_mfma_f32_16x16x32_bf16 v[32:35], v[132:135], v[0:3], 0\n"
        "s_waitcnt lgkmcnt(2)\n"
        "v_cvt_pk_bf16_f32 v84, v20, v21\n"
        "v_cvt_pk_bf16_f32 v85, v22, v23\n"
        "v_mfma_f32_16x16x32_bf16 v[32:35], v[136:139], v[4:7], v[32:35]\n"
        "ds_read_b128 v[0:3], v103 offset:49408\n"
        "v_cvt_pk_bf16_f32 v92, v24, v25\n"
        "v_cvt_pk_bf16_f32 v93, v26, v27\n"
        "v_mfma_f32_16x16x32_bf16 v[32:35], v[140:143], v[8:11], v[32:35]\n"
        "ds_read_b128 v[4:7], v103 offset:49472\n"
        "v_cvt_pk_bf16_f32 v102, v30, v31\n"
        "v_mul_f32 v28, s78, v28\n"
        "v_mfma_f32_16x16x32_bf16 v[36:39], v[132:135], v[40:43], 0\n"
        "ds_read_b128 v[8:11], v103 offset:49536\n"
        "v_mul_f32 v29, s78, v29\n"
        "v_cvt_pk_bf16_f32 v118, v28, v29\n"
        "v_mfma_f32_16x16x32_bf16 v[36:39], v[136:139], v[44:47], v[36:39]\n"
        "buffer_store_dwordx3 v[120:122], v111, s[40:43], s35 offen sc1\n"
        "v_mfma_f32_16x16x32_bf16 v[36:39], v[140:143], v[48:51], v[36:39]\n"
        "s_mul_i32 s56, s56, s83\n"
        "s_mul_i32 s92, s66, 1536\n"
        "s_sub_i32 s75, 0, s75\n"
        "v_mfma_f32_16x16x32_bf16 v[16:19], v[144:147], v[40:43], 0\n"
        "s_mov_b32 s79, 0x49800000\n"
        "v_mul_f32 v32, s78, v32\n"
        "v_mul_f32 v33, s78, v33\n"
        "v_mfma_f32_16x16x32_bf16 v[16:19], v[148:151], v[44:47], v[16:19]\n"
        "s_add_i32 s56, s56, s92\n"
        "v_mul_f32 v34, s78, v34\n"
        "v_mul_f32 v35, s78, v35\n"
        "v_mfma_f32_16x16x32_bf16 v[16:19], v[152:155], v[48:51], v[16:19]\n"
        "s_addk_i32 s56, 768\n"
        "v_cvt_pk_bf16_f32 v116, v32, v33\n"
        "v_cvt_pk_bf16_f32 v117, v34, v35\n"
        "v_mfma_f32_16x16x32_bf16 v[20:23], v[156:159], v[40:43], 0\n"
        "s_cmp_eq_u32 s66, 63\n"
        "s_cselect_b32 s71, s83, 0\n"
        "v_mul_f32 v36, s78, v36\n"
        "v_mul_f32 v37, s78, v37\n"
        "v_mfma_f32_16x16x32_bf16 v[20:23], v[160:163], v[44:47], v[20:23]\n"
        "buffer_store_dwordx3 v[124:126], v111, s[44:47], s34 offen sc1\n"
        "v_mul_f32 v38, s78, v38\n"
        "v_mul_f32 v39, s78, v39\n"
        "v_mfma_f32_16x16x32_bf16 v[20:23], v[164:167], v[48:51], v[20:23]\n"
        "s_sub_i32 s57, s56, s71\n"
        "v_cvt_pk_bf16_f32 v120, v36, v37\n"
        "v_cvt_pk_bf16_f32 v121, v38, v39\n"
        "v_mfma_f32_16x16x32_bf16 v[24:27], v[40:43], v[168:171], 0\n"
        "s_addk_i32 s57, 768\n"
        "v_add_u32 v64, s74, v107\n"
        "v_cvt_pk_bf16_f32 v106, v18, v19\n"
        "v_mfma_f32_16x16x32_bf16 v[24:27], v[44:47], v[172:175], v[24:27]\n"
        "s_waitcnt vmcnt(6)\n"
        "v_mul_f32 v16, s78, v16\n"
        "v_mul_f32 v17, s78, v17\n"
        "v_mfma_f32_16x16x32_bf16 v[24:27], v[48:51], v[176:179], v[24:27]\n"
        "ds_read_b128 v[68:71], v64 offset:1024\n"
        "v_cvt_pk_bf16_f32 v122, v16, v17\n"
        "v_cvt_pk_bf16_f32 v104, v20, v21\n"
        "v_mfma_f32_16x16x32_bf16 v[28:31], v[40:43], v[180:183], v[12:15]\n"
        "ds_read_b128 v[64:67], v64\n"
        "ds_read_b128 v[40:43], v103 offset:52480\n"
        "v_cvt_pk_bf16_f32 v105, v22, v23\n"
        "v_mfma_f32_16x16x32_bf16 v[28:31], v[44:47], v[184:187], v[28:31]\n"
        "s_cmp_lt_u32 s64, 6\n"
        "s_cselect_b32 s56, s56, 1073741824\n"
        "ds_read_b128 v[44:47], v103 offset:52544\n"
        "v_mfma_f32_16x16x32_bf16 v[28:31], v[48:51], v[188:191], v[28:31]\n"
        "s_lshl_b32 s70, s65, 19\n"
        "ds_read_b128 v[48:51], v103 offset:52608\n"
        "s_waitcnt lgkmcnt(5)\n"
        "v_mfma_f32_16x16x32_bf16 v[32:35], v[132:135], v[0:3], 0\n"
        "s_waitcnt lgkmcnt(3)\n"
        "v_or3_b32 v64, v64, v65, v66\n"
        "v_or3_b32 v68, v68, v69, v70\n"
        "v_mfma_f32_16x16x32_bf16 v[32:35], v[136:139], v[4:7], v[32:35]\n"
        "s_lshl_b32 s93, s66, 13\n"
        "v_or3_b32 v64, v64, v67, v71\n"
        "v_or_b32 v64, v64, v68\n"
        "v_mfma_f32_16x16x32_bf16 v[32:35], v[140:143], v[8:11], v[32:35]\n"
        "s_add_i32 s70, s70, s93\n"
        "s_mov_b32 m0, s74\n"
        "v_cmp_ne_u32 vcc, 0, v64\n"
        "v_cvt_pk_bf16_f32 v86, v24, v25\n"
        "v_mfma_f32_16x16x32_bf16 v[36:39], v[144:147], v[0:3], 0\n"
        "s_add_i32 s70, s70, s69\n"
        "s_cmp_lt_u32 s64, 6\n"
        "s_cselect_b32 s70, s70, 1073741824\n"
        "s_add_i32 s65, s65, 8\n"
        "s_and_b32 s65, s65, 63\n"
        "s_add_i32 s66, s68, s65\n"
        "s_and_b32 s66, s66, 63\n"
        "s_cmp_lg_u32 s71, s33\n"
        "s_cbranch_scc1 L_fix%=\n"
        "L_fixdone%=:\n"
        "s_cmp_lg_u64 vcc, 0\n"
        "s_cselect_b32 s95, 1, 0\n"
        "s_or_b32 s55, s95, s98\n"
        "v_cvt_pk_bf16_f32 v87, v26, v27\n"
        "v_xor_b32 v103, 12288, v103\n"
        "v_mfma_f32_16x16x32_bf16 v[36:39], v[148:151], v[4:7], v[36:39]\n"
        "buffer_load_dwordx4 v107, s[36:39], s70 offen lds\n"
        "v_cvt_pk_bf16_f32 v94, v28, v29\n"
        "v_cvt_pk_bf16_f32 v95, v30, v31\n"
        "v_mfma_f32_16x16x32_bf16 v[36:39], v[152:155], v[8:11], v[36:39]\n"
        "buffer_store_dwordx3 v[128:130], v111, s[44:47], s35 offen sc1\n"
        "v_mul_f32 v32, s78, v32\n"
        "v_mul_f32 v33, s78, v33\n"
        "v_mfma_f32_16x16x32_bf16 v[16:19], v[156:159], v[0:3], 0\n"
        "s_waitcnt vmcnt(5)\n"
        "v_mov_b32 v65, s81\n"
        "v_mov_b32 v64, s55\n"
        "v_mfma_f32_16x16x32_bf16 v[16:19], v[160:163], v[4:7], v[16:19]\n"
        "ds_write_b8 v65, v64\n"
        "v_mul_f32 v34, s78, v34\n"
        "v_mul_f32 v35, s78, v35\n"
        "v_mfma_f32_16x16x32_bf16 v[16:19], v[164:167], v[8:11], v[16:19]\n"
        "v_cvt_pk_bf16_f32 v124, v32, v33\n"
        "v_cvt_pk_bf16_f32 v125, v34, v35\n"
        "v_mfma_f32_16x16x32_bf16 v[20:23], v[0:3], v[168:171], 0\n"
        "buffer_load_dwordx4 v107, s[36:39], s70 offen offset:1024 lds\n"
        "v_cvt_pk_bf16_f32 v110, v38, v39\n"
        "v_mul_f32 v36, s78, v36\n"
        "v_mfma_f32_16x16x32_bf16 v[20:23], v[4:7], v[172:175], v[20:23]\n"
        "v_mul_f32 v37, s78, v37\n"
        "v_cvt_pk_bf16_f32 v126, v36, v37\n"
        "v_mfma_f32_16x16x32_bf16 v[20:23], v[8:11], v[176:179], v[20:23]\n"
        "v_cvt_pk_bf16_f32 v108, v16, v17\n"
        "v_cvt_pk_bf16_f32 v109, v18, v19\n"
        "v_mfma_f32_16x16x32_bf16 v[24:27], v[0:3], v[180:183], v[12:15]\n"
        "v_mfma_f32_16x16x32_bf16 v[24:27], v[4:7], v[184:187], v[24:27]\n"
        "v_mfma_f32_16x16x32_bf16 v[24:27], v[8:11], v[188:191], v[24:27]\n"
        "s_waitcnt lgkmcnt(1)\n"
        "v_mfma_f32_16x16x32_bf16 v[28:31], v[156:159], v[40:43], 0\n"
        "v_mov_b32 v74, s82\n"
        "v_mfma_f32_16x16x32_bf16 v[28:31], v[160:163], v[44:47], v[28:31]\n"
        "ds_read_b64 v[74:75], v74\n"
        "v_cvt_pk_bf16_f32 v88, v20, v21\n"
        "v_cvt_pk_bf16_f32 v89, v22, v23\n"
        "v_mfma_f32_16x16x32_bf16 v[28:31], v[164:167], v[48:51], v[28:31]\n"
        "v_mfma_f32_16x16x32_bf16 v[32:35], v[144:147], v[40:43], 0\n"
        "v_mfma_f32_16x16x32_bf16 v[32:35], v[148:151], v[44:47], v[32:35]\n"
        "v_cvt_pk_bf16_f32 v96, v24, v25\n"
        "v_cvt_pk_bf16_f32 v97, v26, v27\n"
        "v_mfma_f32_16x16x32_bf16 v[32:35], v[152:155], v[48:51], v[32:35]\n"
        "v_mfma_f32_16x16x32_bf16 v[36:39], v[40:43], v[168:171], 0\n"
        "s_waitcnt lgkmcnt(0)\n"
        "v_readfirstlane_b32 s77, v74\n"
        "v_readfirstlane_b32 s99, v75\n"
        "v_mfma_f32_16x16x32_bf16 v[36:39], v[44:47], v[172:175], v[36:39]\n"
        "s_bitcmp1_b32 %[b], 8\n"
        "s_nop 1\n"
        "s_cselect_b32 s94, s99, s77\n"
        "s_cselect_b32 s99, s77, s99\n"
        "s_mov_b32 s77, s94\n"
        "s_and_b32 s94, s77, 2122219134\n"
        "s_cmp_lg_u32 s94, s98\n"
        "s_cbranch_scc1 L_sp17%=\n"
        "L_sp17b%=:\n"
        "ds_read_b128 v[52:55], v115 offset:18688\n"
        "ds_read_b128 v[56:59], v115 offset:22784\n"
        "ds_read_b128 v[60:63], v115 offset:26880\n"
        "s_mov_b32 m0, s72\n"
        "v_subrev_u32 v115, s75, v115\n"
        "v_cvt_pk_bf16_f32 v112, v28, v29\n"
        "v_mfma_f32_16x16x32_bf16 v[36:39], v[48:51], v[176:179], v[36:39]\n"
        "buffer_load_dwordx4 v78, s[28:31], s56 offen lds\n"
        "v_cvt_pk_bf16_f32 v113, v30, v31\n"
        "v_cvt_pk_bf16_f32 v114, v34, v35\n"
        "v_mfma_f32_16x16x32_bf16 v[16:19], v[40:43], v[180:183], v[12:15]\n"
        "s_and_b32 s94, s99, 126\n"
        "s_and_b32 s92, s98, 126\n"
        "s_cmp_lt_u32 s94, s92\n"
        "s_cselect_b32 s84, 1, 0\n"
        "v_mul_f32 v32, s78, v32\n"
        "v_mul_f32 v33, s78, v33\n"
        "v_mfma_f32_16x16x32_bf16 v[16:19], v[44:47], v[184:187], v[16:19]\n"
        "ds_read_b128 v[64:67], v107\n"
        "ds_read_b128 v[68:71], v107 offset:1024\n"
        "v_cvt_pk_bf16_f32 v130, v32, v33\n"
        "v_mfma_f32_16x16x32_bf16 v[16:19], v[48:51], v[188:191], v[16:19]\n"
        "v_cvt_pk_bf16_f32 v90, v36, v37\n"
        "v_cvt_pk_bf16_f32 v91, v38, v39\n"
        "v_mfma_f32_16x16x32_bf16 v[32:35], v[132:135], v[40:43], 0\n"
        "v_mfma_f32_16x16x32_bf16 v[0:3], v[100:103], v[116:119], v[192:195]\n"
        "v_mfma_f32_16x16x32_bf16 v[32:35], v[136:139], v[44:47], v[32:35]\n"
        "s_setprio 2\n"
        "s_cmp_eq_u32 s84, 0\n"
        "s_cbranch_scc1 L_p19%=\n"
        "s_setprio 0\n"
        "L_p19%=:\n"
        "v_mfma_f32_16x16x32_bf16 v[4:7], v[104:107], v[116:119], v[196:199]\n"
        "s_mov_b64 s[34:35], s[58:59]\n"
        "s_mov_b64 s[58:59], s[60:61]\n"
        "s_mov_b64 s[60:61], s[62:63]\n"
        "v_mfma_f32_16x16x32_bf16 v[32:35], v[140:143], v[48:51], v[32:35]\n"
        "s_mov_b64 s[62:63], s[56:57]\n"
        "v_mfma_f32_16x16x32_bf16 v[8:11], v[108:111], v[116:119], v[200:203]\n"
        "v_cvt_pk_bf16_f32 v98, v16, v17\n"
        "v_cvt_pk_bf16_f32 v99, v18, v19\n"
        "v_mfma_f32_16x16x32_bf16 v[12:15], v[112:115], v[116:119], v[204:207]\n"
        "v_exp_f32 v0, v0\n"
        "v_exp_f32 v1, v1\n"
        "v_exp_f32 v2, v2\n"
        "v_exp_f32 v3, v3\n"
        "v_mfma_f32_16x16x32_bf16 v[16:19], v[100:103], v[120:123], v[208:211]\n"
        "buffer_load_dwordx4 v79, s[28:31], s56 offen offset:1024 lds\n"
        "v_exp_f32 v4, v4\n"
        "v_exp_f32 v5, v5\n"
        "v_mfma_f32_16x16x32_bf16 v[20:23], v[104:107], v[120:123], v[212:215]\n"
        "ds_read_b128 v[72:75], v107 offset:2048\n"
        "v_exp_f32 v6, v6\n"
        "v_exp_f32 v7, v7\n"
        "v_cvt_pk_bf16_f32 v0, v0, v1\n"
        "v_cvt_pk_bf16_f32 v1, v2, v3\n"
        "v_cvt_pk_bf16_f32 v2, v4, v5\n"
        "v_cvt_pk_bf16_f32 v3, v6, v7\n"
        "v_mul_f32 v32, s78, v32\n"
        "v_mul_f32 v33, s78, v33\n"
        "v_mul_f32 v34, s78, v34\n"
        "v_mul_f32 v35, s78, v35\n"
        "v_cvt_pk_bf16_f32 v128, v32, v33\n"
        "v_cvt_pk_bf16_f32 v129, v34, v35\n"
        "v_mfma_f32_16x16x32_bf16 v[24:27], v[108:111], v[120:123], v[216:219]\n"
        "s_xor_b32 s81, s81, 8\n"
        "s_xor_b32 s82, s82, 8\n"
        "v_mfma_f32_16x16x32_bf16 v[28:31], v[112:115], v[120:123], v[220:223]\n"
        "s_and_b32 s76, s77, 16843009\n"
        "s_add_i32 s98, s98, 33686018\n"
        "v_exp_f32 v8, v8\n"
        "v_mfma_f32_16x16x32_bf16 v[32:35], v[84:87], v[0:3], 0\n"
        "v_exp_f32 v9, v9\n"
        "v_mfma_f32_16x16x32_bf16 v[36:39], v[92:95], v[0:3], 0\n"
        "s_waitcnt lgkmcnt(0)\n"
        "v_exp_f32 v10, v10\n"
        "v_mfma_f32_16x16x32_bf16 v[40:43], v[52:55], v[64:67], 0\n"
        "ds_read_b128 v[64:67], v107 offset:3120\n"
        "v_exp_f32 v11, v11\n"
        "v_exp_f32 v12, v12\n"
        "v_mfma_f32_16x16x32_bf16 v[40:43], v[56:59], v[68:71], v[40:43]\n"
        "ds_read_b128 v[68:71], v107 offset:4144\n"
        "v_exp_f32 v13, v13\n"
        "v_exp_f32 v14, v14\n"
        "v_mfma_f32_16x16x32_bf16 v[40:43], v[60:63], v[72:75], v[40:43]\n"
        "ds_read_b128 v[72:75], v107 offset:5168\n"
        "v_exp_f32 v15, v15\n"
        "v_cvt_pk_bf16_f32 v8, v8, v9\n"
        "v_cvt_pk_bf16_f32 v9, v10, v11\n"
        "v_cvt_pk_bf16_f32 v10, v12, v13\n"
        "v_cvt_pk_bf16_f32 v11, v14, v15\n"
        "v_exp_f32 v16, v16\n"
        "v_exp_f32 v17, v17\n"
        "v_mfma_f32_16x16x32_bf16 v[32:35], v[88:91], v[8:11], v[32:35]\n"
        "s_sub_i32 s72, s73, s72\n"
        "v_exp_f32 v18, v18\n"
        "v_mfma_f32_16x16x32_bf16 v[36:39], v[96:99], v[8:11], v[36:39]\n"
        "buffer_load_dwordx4 v80, s[28:31], s56 offen offset:2048 lds\n"
        "v_exp_f32 v19, v19\n"
        "v_mfma_f32_16x16x32_bf16 v[0:3], v[100:103], v[124:127], v[224:227]\n"
        "v_exp_f32 v20, v20\n"
        "v_exp_f32 v21, v21\n"
        "v_mfma_f32_16x16x32_bf16 v[4:7], v[104:107], v[124:127], v[228:231]\n"
        "s_waitcnt lgkmcnt(0)\n"
        "v_exp_f32 v22, v22\n"
        "v_exp_f32 v23, v23\n"
        "v_mfma_f32_16x16x32_bf16 v[44:47], v[52:55], v[64:67], 0\n"
        "ds_read_b128 v[64:67], v107 offset:6240\n"
        "v_rcp_f32 v39, v38\n"
        "v_cvt_pk_bf16_f32 v16, v16, v17\n"
        "v_cvt_pk_bf16_f32 v17, v18, v19\n"
        "v_mfma_f32_16x16x32_bf16 v[44:47], v[56:59], v[68:71], v[44:47]\n"
        "ds_read_b128 v[68:71], v107 offset:7264\n"
        "v_cvt_pk_bf16_f32 v18, v20, v21\n"
        "v_cvt_pk_bf16_f32 v19, v22, v23\n"
        "v_mul_f32 v32, v39, v32\n"
        "v_mul_f32 v33, v39, v33\n"
        "v_mfma_f32_16x16x32_bf16 v[44:47], v[60:63], v[72:75], v[44:47]\n"
        "ds_read_b128 v[72:75], v107 offset:8288\n"
        "v_mul_f32 v34, v39, v34\n"
        "v_mul_f32 v35, v39, v35\n"
        "v_cvt_pk_bf16_f32 v32, v32, v33\n"
        "v_cvt_pk_bf16_f32 v33, v34, v35\n"
        "v_mfma_f32_16x16x32_bf16 v[8:11], v[108:111], v[124:127], v[232:235]\n"
        "ds_write_b64 v81, v[32:33] offset:18688\n"
        "v_mul_f32 v36, v39, v36\n"
        "v_mul_f32 v37, v39, v37\n"
        "v_cvt_pk_bf16_f32 v36, v36, v37\n"
        "v_max_f32 v76, v38, v39\n"
        "v_mfma_f32_16x16x32_bf16 v[12:15], v[112:115], v[124:127], v[236:239]\n"
        "ds_write_b32 v82, v36 offset:18688\n"
        "v_exp_f32 v24, v24\n"
        "v_mfma_f32_16x16x32_bf16 v[32:35], v[84:87], v[16:19], 0\n"
        "v_exp_f32 v25, v25\n"
        "v_mfma_f32_16x16x32_bf16 v[36:39], v[92:95], v[16:19], 0\n"
        "s_waitcnt lgkmcnt(2)\n"
        "v_exp_f32 v26, v26\n"
        "v_mfma_f32_16x16x32_bf16 v[48:51], v[52:55], v[64:67], 0\n"
        "ds_read_b128 v[64:67], v107 offset:9360\n"
        "v_exp_f32 v27, v27\n"
        "v_exp_f32 v28, v28\n"
        "v_mfma_f32_16x16x32_bf16 v[48:51], v[56:59], v[68:71], v[48:51]\n"
        "ds_read_b128 v[68:71], v107 offset:10384\n"
        "v_exp_f32 v29, v29\n"
        "v_exp_f32 v30, v30\n"
        "v_mfma_f32_16x16x32_bf16 v[48:51], v[60:63], v[72:75], v[48:51]\n"
        "ds_read_b128 v[72:75], v107 offset:11408\n"
        "v_exp_f32 v31, v31\n"
        "v_cvt_pk_bf16_f32 v24, v24, v25\n"
        "v_cvt_pk_bf16_f32 v25, v26, v27\n"
        "v_cvt_pk_bf16_f32 v26, v28, v29\n"
        "v_cvt_pk_bf16_f32 v27, v30, v31\n"
        "v_exp_f32 v0, v0\n"
        "v_exp_f32 v1, v1\n"
        "v_mfma_f32_16x16x32_bf16 v[32:35], v[88:91], v[24:27], v[32:35]\n"
        "v_exp_f32 v2, v2\n"
        "v_mfma_f32_16x16x32_bf16 v[36:39], v[96:99], v[24:27], v[36:39]\n"
        "v_exp_f32 v3, v3\n"
        "v_mfma_f32_16x16x32_bf16 v[16:19], v[100:103], v[128:131], v[240:243]\n"
        "v_exp_f32 v4, v4\n"
        "v_exp_f32 v5, v5\n"
        "v_mfma_f32_16x16x32_bf16 v[20:23], v[104:107], v[128:131], v[244:247]\n"
        "v_exp_f32 v6, v6\n"
        "v_exp_f32 v7, v7\n"
        "s_nop 0\n"
        "v_rcp_f32 v39, v38\n"
        "v_cvt_pk_bf16_f32 v0, v0, v1\n"
        "v_cvt_pk_bf16_f32 v1, v2, v3\n"
        "v_cvt_pk_bf16_f32 v2, v4, v5\n"
        "v_cvt_pk_bf16_f32 v3, v6, v7\n"
        "v_mul_f32 v32, v39, v32\n"
        "v_mul_f32 v33, v39, v33\n"
        "v_mul_f32 v34, v39, v34\n"
        "v_mul_f32 v35, v39, v35\n"
        "v_cvt_pk_bf16_f32 v32, v32, v33\n"
        "v_cvt_pk_bf16_f32 v33, v34, v35\n"
        "v_mfma_f32_16x16x32_bf16 v[24:27], v[108:111], v[128:131], v[248:251]\n"
        "ds_write_b64 v81, v[32:33] offset:18944\n"
        "v_mul_f32 v36, v39, v36\n"
        "v_mul_f32 v37, v39, v37\n"
        "v_cvt_pk_bf16_f32 v36, v36, v37\n"
        "v_max3_f32 v76, v76, v38, v39\n"
        "v_mfma_f32_16x16x32_bf16 v[28:31], v[112:115], v[128:131], v[252:255]\n"
        "ds_write_b32 v82, v36 offset:18944\n"
        "v_cvt_pk_bf16_f32 v116, v40, v44\n"
        "v_cvt_pk_bf16_f32 v120, v41, v45\n"
        "v_cvt_pk_bf16_f32 v124, v42, v46\n"
        "v_cvt_pk_bf16_f32 v128, v43, v47\n"
        "v_exp_f32 v8, v8\n"
        "v_mfma_f32_16x16x32_bf16 v[32:35], v[84:87], v[0:3], 0\n"
        "v_exp_f32 v9, v9\n"
        "v_mfma_f32_16x16x32_bf16 v[36:39], v[92:95], v[0:3], 0\n"
        "s_waitcnt lgkmcnt(2)\n"
        "v_exp_f32 v10, v10\n"
        "v_mfma_f32_16x16x32_bf16 v[40:43], v[52:55], v[64:67], 0\n"
        "ds_read_b128 v[64:67], v107 offset:12480\n"
        "v_exp_f32 v11, v11\n"
        "v_exp_f32 v12, v12\n"
        "v_mfma_f32_16x16x32_bf16 v[40:43], v[56:59], v[68:71], v[40:43]\n"
        "ds_read_b128 v[68:71], v107 offset:13504\n"
        "v_exp_f32 v13, v13\n"
        "v_exp_f32 v14, v14\n"
        "v_mfma_f32_16x16x32_bf16 v[40:43], v[60:63], v[72:75], v[40:43]\n"
        "ds_read_b128 v[72:75], v107 offset:14528\n"
        "v_exp_f32 v15, v15\n"
        "v_cvt_pk_bf16_f32 v8, v8, v9\n"
        "v_cvt_pk_bf16_f32 v9, v10, v11\n"
        "v_cvt_pk_bf16_f32 v10, v12, v13\n"
        "v_cvt_pk_bf16_f32 v11, v14, v15\n"
        "v_exp_f32 v16, v16\n"
        "v_exp_f32 v17, v17\n"
        "v_mfma_f32_16x16x32_bf16 v[32:35], v[88:91], v[8:11], v[32:35]\n"
        "ds_read_b128 v[0:3], v103 offset:43264\n"
        "v_exp_f32 v18, v18\n"
        "v_mfma_f32_16x16x32_bf16 v[36:39], v[96:99], v[8:11], v[36:39]\n"
        "ds_read_b128 v[4:7], v103 offset:43328\n"
        "s_waitcnt lgkmcnt(2)\n"
        "v_exp_f32 v19, v19\n"
        "v_exp_f32 v20, v20\n"
        "v_exp_f32 v21, v21\n"
        "v_exp_f32 v22, v22\n"
        "v_exp_f32 v23, v23\n"
        "v_cvt_pk_bf16_f32 v16, v16, v17\n"
        "v_cvt_pk_bf16_f32 v17, v18, v19\n"
        "v_mfma_f32_16x16x32_bf16 v[44:47], v[52:55], v[64:67], 0\n"
        "ds_read_b128 v[8:11], v103 offset:43392\n"
        "v_rcp_f32 v39, v38\n"
        "v_cvt_pk_bf16_f32 v18, v20, v21\n"
        "v_cvt_pk_bf16_f32 v19, v22, v23\n"
        "v_mul_f32 v32, v39, v32\n"
        "v_mul_f32 v33, v39, v33\n"
        "v_mul_f32 v34, v39, v34\n"
        "v_mul_f32 v35, v39, v35\n"
        "v_cvt_pk_bf16_f32 v32, v32, v33\n"
        "v_cvt_pk_bf16_f32 v33, v34, v35\n"
        "v_mfma_f32_16x16x32_bf16 v[44:47], v[56:59], v[68:71], v[44:47]\n"
        "ds_read_b128 v[64:67], v107 offset:15600\n"
        "ds_write_b64 v81, v[32:33] offset:19200\n"
        "v_mul_f32 v36, v39, v36\n"
        "v_mul_f32 v37, v39, v37\n"
        "v_cvt_pk_bf16_f32 v36, v36, v37\n"
        "v_max3_f32 v76, v76, v38, v39\n"
        "v_mfma_f32_16x16x32_bf16 v[44:47], v[60:63], v[72:75], v[44:47]\n"
        "ds_read_b128 v[68:71], v107 offset:16624\n"
        "ds_write_b32 v82, v36 offset:19200\n"
        "v_cvt_pk_bf16_f32 v117, v48, v40\n"
        "v_cvt_pk_bf16_f32 v121, v49, v41\n"
        "v_cvt_pk_bf16_f32 v125, v50, v42\n"
        "v_cvt_pk_bf16_f32 v129, v51, v43\n"
        "v_exp_f32 v24, v24\n"
        "v_mfma_f32_16x16x32_bf16 v[32:35], v[84:87], v[16:19], 0\n"
        "ds_read_b128 v[72:75], v107 offset:17648\n"
        "v_exp_f32 v25, v25\n"
        "v_mfma_f32_16x16x32_bf16 v[36:39], v[92:95], v[16:19], 0\n"
        "s_waitcnt lgkmcnt(2)\n"
        "v_exp_f32 v26, v26\n"
        "v_mfma_f32_16x16x32_bf16 v[48:51], v[52:55], v[64:67], 0\n"
        "v_exp_f32 v27, v27\n"
        "v_exp_f32 v28, v28\n"
        "v_mfma_f32_16x16x32_bf16 v[48:51], v[56:59], v[68:71], v[48:51]\n"
        "s_waitcnt lgkmcnt(0)\n"
        "v_exp_f32 v29, v29\n"
        "v_exp_f32 v30, v30\n"
        "v_mfma_f32_16x16x32_bf16 v[48:51], v[60:63], v[72:75], v[48:51]\n"
        "v_exp_f32 v31, v31\n"
        "v_cvt_pk_bf16_f32 v24, v24, v25\n"
        "v_cvt_pk_bf16_f32 v25, v26, v27\n"
        "v_cvt_pk_bf16_f32 v26, v28, v29\n"
        "v_cvt_pk_bf16_f32 v27, v30, v31\n"
        "s_add_i32 s64, s64, 1\n"
        "s_cmp_lt_u32 s64, 8\n"
        "v_mfma_f32_16x16x32_bf16 v[32:35], v[88:91], v[24:27], v[32:35]\n"
        "v_mfma_f32_16x16x32_bf16 v[36:39], v[96:99], v[24:27], v[36:39]\n"
        "L_back%=:\n"
        "s_cbranch_scc1 L_top%=\n"
        "s_mov_b32 s55, s98\n"
        "ds_read_b128 v[0:3], v107\n"
        "ds_read_b128 v[4:7], v107 offset:1024\n"
        "ds_read_b128 v[8:11], v107 offset:2048\n"
        "ds_read_b128 v[12:15], v107 offset:3120\n"
        "ds_read_b128 v[64:67], v107 offset:4144\n"
        "ds_read_b128 v[68:71], v107 offset:5168\n"
        "ds_read_b128 v[72:75], v107 offset:6240\n"
        "v_rcp_f32 v39, v38\n"
        "v_cvt_pk_bf16_f32 v118, v44, v48\n"
        "v_max3_f32 v76, v76, v38, v39\n"
        "v_cvt_pk_bf16_f32 v122, v45, v49\n"
        "v_cmp_lt_f32 vcc, s79, v76\n"
        "v_cvt_pk_bf16_f32 v126, v46, v50\n"
        "v_cvt_pk_bf16_f32 v130, v47, v51\n"
        "v_mov_b32 v53, s81\n"
        "v_mov_b32 v52, s55\n"
        "s_cbranch_vccnz L_redo%=\n"
        "v_mul_f32 v32, v39, v32\n"
        "v_mul_f32 v33, v39, v33\n"
        "v_mul_f32 v34, v39, v34\n"
        "v_mul_f32 v35, v39, v35\n"
        "v_cvt_pk_bf16_f32 v32, v32, v33\n"
        "v_cvt_pk_bf16_f32 v33, v34, v35\n"
        "ds_write_b64 v81, v[32:33] offset:19456\n"
        "v_mul_f32 v36, v39, v36\n"
        "v_mul_f32 v37, v39, v37\n"
        "v_cvt_pk_bf16_f32 v36, v36, v37\n"
        "ds_write_b32 v82, v36 offset:19456\n"
        "ds_write_b8 v53, v52\n"
        "v_mov_b32 v54, s82\n"
        "ds_read_b64 v[54:55], v54\n"
        "ds_read_b128 v[40:43], v107 offset:7264\n"
        "ds_read_b128 v[44:47], v107 offset:8288\n"
        "buffer_store_dwordx3 v[116:118], v111, s[40:43], s34 offen sc1\n"
        "buffer_store_dwordx3 v[120:122], v111, s[40:43], s35 offen sc1\n"
        "buffer_store_dwordx3 v[124:126], v111, s[44:47], s34 offen sc1\n"
        "buffer_store_dwordx3 v[128:130], v111, s[44:47], s35 offen sc1\n"
        "s_waitcnt lgkmcnt(2)\n"
        "v_readfirstlane_b32 s77, v54\n"
        "v_readfirstlane_b32 s99, v55\n"
        "s_bitcmp1_b32 %[b], 8\n"
        "s_nop 2\n"
        "s_cselect_b32 s94, s99, s77\n"
        "s_cselect_b32 s99, s77, s99\n"
        "s_mov_b32 s77, s94\n"
        "s_and_b32 s94, s77, 2122219134\n"
        "s_cmp_lg_u32 s94, s98\n"
        "s_cbranch_scc1 L_sp20%=\n"
        "L_sp20b%=:\n"
        "ds_read_b128 v[52:55], v115 offset:18688\n"
        "ds_read_b128 v[56:59], v115 offset:22784\n"
        "ds_read_b128 v[60:63], v115 offset:26880\n"
        "s_waitcnt lgkmcnt(0)\n"
        "v_mfma_f32_16x16x32_bf16 v[16:19], v[52:55], v[0:3], 0\n"
        "ds_read_b128 v[0:3], v107 offset:9360\n"
        "v_mfma_f32_16x16x32_bf16 v[16:19], v[56:59], v[4:7], v[16:19]\n"
        "ds_read_b128 v[4:7], v107 offset:10384\n"
        "v_mfma_f32_16x16x32_bf16 v[16:19], v[60:63], v[8:11], v[16:19]\n"
        "ds_read_b128 v[8:11], v107 offset:11408\n"
        "v_mfma_f32_16x16x32_bf16 v[20:23], v[52:55], v[12:15], 0\n"
        "ds_read_b128 v[12:15], v107 offset:12480\n"
        "v_mfma_f32_16x16x32_bf16 v[20:23], v[56:59], v[64:67], v[20:23]\n"
        "ds_read_b128 v[64:67], v107 offset:13504\n"
        "v_mfma_f32_16x16x32_bf16 v[20:23], v[60:63], v[68:71], v[20:23]\n"
        "ds_read_b128 v[68:71], v107 offset:14528\n"
        "v_mfma_f32_16x16x32_bf16 v[24:27], v[52:55], v[72:75], 0\n"
        "ds_read_b128 v[72:75], v107 offset:15600\n"
        "v_mfma_f32_16x16x32_bf16 v[24:27], v[56:59], v[40:43], v[24:27]\n"
        "ds_read_b128 v[40:43], v107 offset:16624\n"
        "v_mfma_f32_16x16x32_bf16 v[24:27], v[60:63], v[44:47], v[24:27]\n"
        "ds_read_b128 v[44:47], v107 offset:17648\n"
        "s_waitcnt lgkmcnt(6)\n"
        "v_mfma_f32_16x16x32_bf16 v[28:31], v[52:55], v[0:3], 0\n"
        "v_mfma_f32_16x16x32_bf16 v[28:31], v[56:59], v[4:7], v[28:31]\n"
        "v_mfma_f32_16x16x32_bf16 v[28:31], v[60:63], v[8:11], v[28:31]\n"
        "s_waitcnt lgkmcnt(3)\n"
        "v_cvt_pk_bf16_f32 v116, v16, v20\n"
        "v_cvt_pk_bf16_f32 v120, v17, v21\n"
        "v_cvt_pk_bf16_f32 v124, v18, v22\n"
        "v_cvt_pk_bf16_f32 v128, v19, v23\n"
        "v_mfma_f32_16x16x32_bf16 v[48:51], v[52:55], v[12:15], 0\n"
        "v_mfma_f32_16x16x32_bf16 v[48:51], v[56:59], v[64:67], v[48:51]\n"
        "v_mfma_f32_16x16x32_bf16 v[48:51], v[60:63], v[68:71], v[48:51]\n"
        "s_waitcnt lgkmcnt(0)\n"
        "v_mfma_f32_16x16x32_bf16 v[16:19], v[52:55], v[72:75], 0\n"
        "v_mfma_f32_16x16x32_bf16 v[16:19], v[56:59], v[40:43], v[16:19]\n"
        "v_mfma_f32_16x16x32_bf16 v[16:19], v[60:63], v[44:47], v[16:19]\n"
        "v_cvt_pk_bf16_f32 v117, v24, v28\n"
        "v_cvt_pk_bf16_f32 v121, v25, v29\n"
        "v_cvt_pk_bf16_f32 v125, v26, v30\n"
        "v_cvt_pk_bf16_f32 v129, v27, v31\n"
        "s_nop 3\n"
        "v_cvt_pk_bf16_f32 v118, v48, v16\n"
        "buffer_store_dwordx3 v[116:118], v111, s[40:43], s58 offen sc1\n"
        "v_cvt_pk_bf16_f32 v122, v49, v17\n"
        "buffer_store_dwordx3 v[120:122], v111, s[40:43], s59 offen sc1\n"
        "v_cvt_pk_bf16_f32 v126, v50, v18\n"
        "buffer_store_dwordx3 v[124:126], v111, s[44:47], s58 offen sc1\n"
        "v_cvt_pk_bf16_f32 v130, v51, v19\n"
        "buffer_store_dwordx3 v[128:130], v111, s[44:47], s59 offen sc1\n"
        "s_branch L_end%=\n"
        "L_fix%=:\n"
        "s_sub_i32 s94, s71, s33\n"
        "s_mov_b32 s33, s71\n"
        "s_mov_b64 exec, s[86:87]\n"
        "v_subrev_u32 v78, s94, v78\n"
        "s_mov_b64 exec, s[88:89]\n"
        "v_subrev_u32 v79, s94, v79\n"
        "s_mov_b64 exec, s[90:91]\n"
        "v_subrev_u32 v80, s94, v80\n"
        "s_mov_b64 exec, -1\n"
        "s_branch L_fixdone%=\n"
        "L_masked%=:\n"
        "s_and_b32 s49, s81, 7\n"
        "s_add_i32 s49, s49, 163640\n"
        "s_and_b32 s50, s49, 4294967292\n"
        "v_mov_b32 v77, s50\n"
        "ds_read_b32 v77, v77\n"
        "v_cvt_pk_bf16_f32 v118, v44, v48\n"
        "v_cvt_pk_bf16_f32 v122, v45, v49\n"
        "v_cvt_pk_bf16_f32 v126, v46, v50\n"
        "v_cvt_pk_bf16_f32 v130, v47, v51\n"
        "ds_read_b128 v[40:43], v103 offset:46336\n"
        "ds_read_b128 v[44:47], v103 offset:46400\n"
        "ds_read_b128 v[48:51], v103 offset:46464\n"
        "v_rcp_f32 v39, v38\n"
        "s_nop 0\n"
        "v_max3_f32 v76, v76, v38, v39\n"
        "v_cmp_lt_f32 vcc, s79, v76\n"
        "s_nop 3\n"
        "s_cbranch_vccnz L_redo%=\n"
        "s_waitcnt lgkmcnt(3)\n"
        "v_readfirstlane_b32 s95, v77\n"
        "s_nop 3\n"
        "s_and_b32 s94, s95, 2122219134\n"
        "s_cmp_lg_u32 s94, s48\n"
        "s_cbranch_scc1 L_sp21%=\n"
        "L_sp21b%=:\n"
        "s_add_i32 s96, s64, 0\n"
        "s_lshl3_add_u32 s96, s96, s85\n"
        "s_and_b32 s96, s96, 63\n"
        "s_add_i32 s95, s68, s96\n"
        "s_and_b32 s95, s95, 63\n"
        "s_lshl_b32 s97, s96, 19\n"
        "s_lshl_b32 s93, s95, 13\n"
        "s_add_i32 s97, s97, s93\n"
        "s_add_i32 s97, s97, s69\n"
        "s_lshr_b32 s51, %[b], 8\n"
        "s_lshl_b32 s51, s51, 13\n"
        "s_add_i32 s51, s51, 141568\n"
        "s_lshl_b32 s94, %[wid], 11\n"
        "s_add_i32 m0, s51, s94\n"
        "v_lshrrev_b32 v54, 4, v107\n"
        "v_and_b32 v54, 48, v54\n"
        "v_xor_b32 v54, v54, v107\n"
        "v_xor_b32 v55, 64, v54\n"
        "buffer_load_dwordx4 v54, s[36:39], s97 offen lds\n"
        "buffer_load_dwordx4 v55, s[36:39], s97 offen offset:1024 lds\n"
        "v_mfma_f32_16x16x32_bf16 v[16:19], v[156:159], v[0:3], 0\n"
        "s_setprio 3\n"
        "s_cmp_eq_u32 s84, 0\n"
        "s_cbranch_scc1 L_p25%=\n"
        "s_setprio 1\n"
        "L_p25%=:\n"
        "v_cndmask_b32 v12, 0, 1.0, s[24:25]\n"
        "v_cndmask_b32 v13, 0, 1.0, s[24:25]\n"
        "v_mfma_f32_16x16x32_bf16 v[16:19], v[160:163], v[4:7], v[16:19]\n"
        "v_cndmask_b32 v14, 0, 1.0, s[24:25]\n"
        "v_cndmask_b32 v15, 0, 1.0, s[24:25]\n"
        "v_mfma_f32_16x16x32_bf16 v[16:19], v[164:167], v[8:11], v[16:19]\n"
        "v_mul_f32 v32, v39, v32\n"
        "v_mul_f32 v33, v39, v33\n"
        "v_mfma_f32_16x16x32_bf16 v[20:23], v[0:3], v[168:171], 0\n"
        "s_mul_i32 s92, s66, 1536\n"
        "v_mul_f32 v34, v39, v34\n"
        "v_mul_f32 v35, v39, v35\n"
        "v_mfma_f32_16x16x32_bf16 v[20:23], v[4:7], v[172:175], v[20:23]\n"
        "buffer_store_dwordx3 v[116:118], v111, s[40:43], s34 offen sc1\n"
        "v_cvt_pk_bf16_f32 v32, v32, v33\n"
        "v_cvt_pk_bf16_f32 v33, v34, v35\n"
        "v_mfma_f32_16x16x32_bf16 v[20:23], v[8:11], v[176:179], v[20:23]\n"
        "ds_write_b64 v81, v[32:33] offset:19456\n"
        "v_mul_f32 v36, v39, v36\n"
        "v_mul_f32 v37, v39, v37\n"
        "v_mfma_f32_16x16x32_bf16 v[24:27], v[0:3], v[180:183], v[12:15]\n"
        "buffer_store_dwordx3 v[120:122], v111, s[40:43], s35 offen sc1\n"
        "v_cvt_pk_bf16_f32 v36, v36, v37\n"
        "v_add_u32 v81, s75, v81\n"
        "v_mfma_f32_16x16x32_bf16 v[24:27], v[4:7], v[184:187], v[24:27]\n"
        "ds_write_b32 v82, v36 offset:19456\n"
        "v_add_u32 v82, s75, v82\n"
        "v_and_b32 v52, 240, v107\n"
        "v_mfma_f32_16x16x32_bf16 v[24:27], v[8:11], v[188:191], v[24:27]\n"
        "s_sub_i32 s75, 0, s75\n"
        "v_lshlrev_b32 v52, 3, v52\n"
        "v_lshrrev_b32 v53, 5, v107\n"
        "v_mfma_f32_16x16x32_bf16 v[28:31], v[144:147], v[0:3], 0\n"
        "s_mov_b32 s79, 0x49800000\n"
        "v_cvt_pk_bf16_f32 v100, v16, v17\n"
        "v_cvt_pk_bf16_f32 v101, v18, v19\n"
        "v_mfma_f32_16x16x32_bf16 v[28:31], v[148:151], v[4:7], v[28:31]\n"
        "buffer_store_dwordx3 v[124:126], v111, s[44:47], s34 offen sc1\n"
        "v_and_b32 v53, 24, v53\n"
        "v_lshrrev_b32 v54, 1, v107\n"
        "v_mfma_f32_16x16x32_bf16 v[28:31], v[152:155], v[8:11], v[28:31]\n"
        "s_lshl3_add_u32 s56, s65, s67\n"
        "s_and_b32 s56, s56, 511\n"
        "s_mul_i32 s56, s56, s83\n"
        "s_add_i32 s56, s56, s92\n"
        "s_addk_i32 s56, 768\n"
        "s_cmp_eq_u32 s66, 63\n"
        "s_cselect_b32 s71, s83, 0\n"
        "s_sub_i32 s57, s56, s71\n"
        "s_addk_i32 s57, 768\n"
        "s_cmp_lt_u32 s64, 6\n"
        "s_cselect_b32 s56, s56, 1073741824\n"
        "s_lshl_b32 s70, s65, 19\n"
        "s_lshl_b32 s93, s66, 13\n"
        "s_add_i32 s70, s70, s93\n"
        "s_add_i32 s70, s70, s69\n"
        "s_cmp_lt_u32 s64, 6\n"
        "s_cselect_b32 s70, s70, 1073741824\n"
        "s_add_i32 s65, s65, 8\n"
        "s_and_b32 s65, s65, 63\n"
        "s_add_i32 s66, s68, s65\n"
        "s_and_b32 s66, s66, 63\n"
        "v_and_b32 v54, 112, v54\n"
        "v_xor_b32 v53, v53, v54\n"
        "s_sub_i32 s94, s71, s33\n"
        "s_mov_b32 s33, s71\n"
        "s_mov_b64 exec, s[86:87]\n"
        "v_subrev_u32 v78, s94, v78\n"
        "s_mov_b64 exec, s[88:89]\n"
        "v_subrev_u32 v79, s94, v79\n"
        "s_mov_b64 exec, s[90:91]\n"
        "v_subrev_u32 v80, s94, v80\n"
        "s_mov_b64 exec, -1\n"
        "v_mfma_f32_16x16x32_bf16 v[32:35], v[132:135], v[0:3], 0\n"
        "s_waitcnt lgkmcnt(2)\n"
        "v_cvt_pk_bf16_f32 v84, v20, v21\n"
        "v_cvt_pk_bf16_f32 v85, v22, v23\n"
        "v_mfma_f32_16x16x32_bf16 v[32:35], v[136:139], v[4:7], v[32:35]\n"
        "ds_read_b128 v[0:3], v103 offset:49408\n"
        "v_add3_u32 v52, v52, v53, s51\n"
        "v_xor_b32 v53, 32, v52\n"
        "v_mfma_f32_16x16x32_bf16 v[32:35], v[140:143], v[8:11], v[32:35]\n"
        "ds_read_b128 v[4:7], v103 offset:49472\n"
        "v_xor_b32 v54, 64, v52\n"
        "v_xor_b32 v55, 96, v52\n"
        "v_mfma_f32_16x16x32_bf16 v[36:39], v[132:135], v[40:43], 0\n"
        "ds_read_b128 v[8:11], v103 offset:49536\n"
        "v_cvt_pk_bf16_f32 v92, v24, v25\n"
        "v_cvt_pk_bf16_f32 v93, v26, v27\n"
        "v_mfma_f32_16x16x32_bf16 v[36:39], v[136:139], v[44:47], v[36:39]\n"
        "buffer_store_dwordx3 v[128:130], v111, s[44:47], s35 offen sc1\n"
        "v_mov_b32 v56, 0\n"
        "v_mov_b32 v57, 0\n"
        "v_mfma_f32_16x16x32_bf16 v[36:39], v[140:143], v[48:51], v[36:39]\n"
        "v_mov_b32 v58, 0\n"
        "v_mov_b32 v59, 0\n"
        "v_mfma_f32_16x16x32_bf16 v[16:19], v[144:147], v[40:43], 0\n"
        "v_cvt_pk_bf16_f32 v102, v30, v31\n"
        "v_mul_f32 v28, s78, v28\n"
        "v_mfma_f32_16x16x32_bf16 v[16:19], v[148:151], v[44:47], v[16:19]\n"
        "v_mul_f32 v29, s78, v29\n"
        "v_cvt_pk_bf16_f32 v118, v28, v29\n"
        "v_mfma_f32_16x16x32_bf16 v[16:19], v[152:155], v[48:51], v[16:19]\n"
        "s_mov_b32 exec_lo, 1048577\n"
        "s_mov_b32 exec_hi, 268435712\n"
        "v_mov_b32 v56, 16313\n"
        "v_mov_b32 v58, 47916\n"
        "s_mov_b64 exec, -1\n"
        "s_mov_b32 exec_lo, 2097154\n"
        "s_mov_b32 exec_hi, 536871424\n"
        "v_mov_b32 v56, 1069088768\n"
        "v_mov_b32 v58, 3140222976\n"
        "s_mov_b64 exec, -1\n"
        "v_mfma_f32_16x16x32_bf16 v[20:23], v[156:159], v[40:43], 0\n"
        "v_mul_f32 v32, s78, v32\n"
        "v_mul_f32 v33, s78, v33\n"
        "v_mfma_f32_16x16x32_bf16 v[20:23], v[160:163], v[44:47], v[20:23]\n"
        "v_mul_f32 v34, s78, v34\n"
        "v_mul_f32 v35, s78, v35\n"
        "v_mfma_f32_16x16x32_bf16 v[20:23], v[164:167], v[48:51], v[20:23]\n"
        "v_cvt_pk_bf16_f32 v116, v32, v33\n"
        "v_cvt_pk_bf16_f32 v117, v34, v35\n"
        "v_mfma_f32_16x16x32_bf16 v[24:27], v[40:43], v[168:171], 0\n"
        "v_mul_f32 v36, s78, v36\n"
        "v_mul_f32 v37, s78, v37\n"
        "v_mfma_f32_16x16x32_bf16 v[24:27], v[44:47], v[172:175], v[24:27]\n"
        "v_mul_f32 v38, s78, v38\n"
        "v_mul_f32 v39, s78, v39\n"
        "v_mfma_f32_16x16x32_bf16 v[24:27], v[48:51], v[176:179], v[24:27]\n"
        "v_cvt_pk_bf16_f32 v120, v36, v37\n"
        "v_cvt_pk_bf16_f32 v121, v38, v39\n"
        "v_mfma_f32_16x16x32_bf16 v[28:31], v[40:43], v[180:183], v[12:15]\n"
        "s_waitcnt lgkmcnt(0)\n"
        "v_cvt_pk_bf16_f32 v106, v18, v19\n"
        "v_mul_f32 v16, s78, v16\n"
        "v_mfma_f32_16x16x32_bf16 v[28:31], v[44:47], v[184:187], v[28:31]\n"
        "ds_read_b128 v[40:43], v103 offset:52480\n"
        "v_mul_f32 v17, s78, v17\n"
        "v_cvt_pk_bf16_f32 v122, v16, v17\n"
        "v_mfma_f32_16x16x32_bf16 v[28:31], v[48:51], v[188:191], v[28:31]\n"
        "ds_read_b128 v[44:47], v103 offset:52544\n"
        "s_mov_b32 exec_lo, 4194308\n"
        "s_mov_b32 exec_hi, 1073742848\n"
        "v_mov_b32 v57, 16313\n"
        "v_mov_b32 v59, 47916\n"
        "s_mov_b64 exec, -1\n"
        "s_mov_b32 exec_lo, 8388616\n"
        "s_mov_b32 exec_hi, 2147485696\n"
        "v_mov_b32 v57, 1069088768\n"
        "v_mov_b32 v59, 3140222976\n"
        "s_mov_b64 exec, -1\n"
        "v_mfma_f32_16x16x32_bf16 v[32:35], v[132:135], v[0:3], 0\n"
        "ds_read_b128 v[48:51], v103 offset:52608\n"
        "v_cvt_pk_bf16_f32 v104, v20, v21\n"
        "v_cvt_pk_bf16_f32 v105, v22, v23\n"
        "v_mfma_f32_16x16x32_bf16 v[32:35], v[136:139], v[4:7], v[32:35]\n"
        "v_add_u32 v64, s74, v107\n"
        "s_waitcnt vmcnt(9)\n"
        "ds_read_b128 v[68:71], v64 offset:1024\n"
        "ds_read_b128 v[64:67], v64\n"
        "s_waitcnt lgkmcnt(0)\n"
        "v_or3_b32 v64, v64, v65, v66\n"
        "v_mfma_f32_16x16x32_bf16 v[32:35], v[140:143], v[8:11], v[32:35]\n"
        "v_or3_b32 v68, v68, v69, v70\n"
        "v_or3_b32 v64, v64, v67, v71\n"
        "s_mov_b32 m0, s74\n"
        "v_mfma_f32_16x16x32_bf16 v[36:39], v[144:147], v[0:3], 0\n"
        "buffer_load_dwordx4 v107, s[36:39], s70 offen lds\n"
        "v_cvt_pk_bf16_f32 v86, v24, v25\n"
        "v_cvt_pk_bf16_f32 v87, v26, v27\n"
        "v_mfma_f32_16x16x32_bf16 v[36:39], v[148:151], v[4:7], v[36:39]\n"
        "buffer_load_dwordx4 v107, s[36:39], s70 offen offset:1024 lds\n"
        "v_or_b32 v64, v64, v68\n"
        "v_cmp_ne_u32 vcc, 0, v64\n"
        "v_mfma_f32_16x16x32_bf16 v[36:39], v[152:155], v[8:11], v[36:39]\n"
        "s_nop 2\n"
        "s_cmp_lg_u64 vcc, 0\n"
        "s_cselect_b32 s95, 1, 0\n"
        "s_or_b32 s55, s95, s98\n"
        "v_xor_b32 v103, 12288, v103\n"
        "v_cvt_pk_bf16_f32 v94, v28, v29\n"
        "v_mfma_f32_16x16x32_bf16 v[16:19], v[156:159], v[0:3], 0\n"
        "v_cvt_pk_bf16_f32 v95, v30, v31\n"
        "v_mov_b32 v65, s81\n"
        "v_mfma_f32_16x16x32_bf16 v[16:19], v[160:163], v[4:7], v[16:19]\n"
        "s_waitcnt vmcnt(6)\n"
        "v_mov_b32 v64, s55\n"
        "v_mul_f32 v32, s78, v32\n"
        "v_mfma_f32_16x16x32_bf16 v[16:19], v[164:167], v[8:11], v[16:19]\n"
        "ds_write_b8 v65, v64\n"
        "v_mul_f32 v33, s78, v33\n"
        "v_mul_f32 v34, s78, v34\n"
        "v_mfma_f32_16x16x32_bf16 v[20:23], v[0:3], v[168:171], 0\n"
        "v_mul_f32 v35, s78, v35\n"
        "v_cvt_pk_bf16_f32 v124, v32, v33\n"
        "v_mfma_f32_16x16x32_bf16 v[20:23], v[4:7], v[172:175], v[20:23]\n"
        "v_cvt_pk_bf16_f32 v125, v34, v35\n"
        "v_cvt_pk_bf16_f32 v110, v38, v39\n"
        "v_mfma_f32_16x16x32_bf16 v[20:23], v[8:11], v[176:179], v[20:23]\n"
        "v_mul_f32 v36, s78, v36\n"
        "v_mul_f32 v37, s78, v37\n"
        "v_mfma_f32_16x16x32_bf16 v[24:27], v[0:3], v[180:183], v[12:15]\n"
        "v_cvt_pk_bf16_f32 v126, v36, v37\n"
        "v_mov_b32 v74, s82\n"
        "v_mfma_f32_16x16x32_bf16 v[24:27], v[4:7], v[184:187], v[24:27]\n"
        "ds_read_b64 v[74:75], v74\n"
        "v_cvt_pk_bf16_f32 v108, v16, v17\n"
        "v_cvt_pk_bf16_f32 v109, v18, v19\n"
        "v_mfma_f32_16x16x32_bf16 v[24:27], v[8:11], v[188:191], v[24:27]\n"
        "v_cvt_pk_bf16_f32 v88, v20, v21\n"
        "v_cvt_pk_bf16_f32 v89, v22, v23\n"
        "v_mfma_f32_16x16x32_bf16 v[28:31], v[156:159], v[40:43], 0\n"
        "v_mfma_f32_16x16x32_bf16 v[28:31], v[160:163], v[44:47], v[28:31]\n"
        "v_mfma_f32_16x16x32_bf16 v[28:31], v[164:167], v[48:51], v[28:31]\n"
        "v_mfma_f32_16x16x32_bf16 v[32:35], v[144:147], v[40:43], 0\n"
        "s_waitcnt lgkmcnt(0)\n"
        "v_readfirstlane_b32 s77, v74\n"
        "v_readfirstlane_b32 s99, v75\n"
        "v_mfma_f32_16x16x32_bf16 v[32:35], v[148:151], v[44:47], v[32:35]\n"
        "s_bitcmp1_b32 %[b], 8\n"
        "s_nop 1\n"
        "s_cselect_b32 s94, s99, s77\n"
        "s_cselect_b32 s99, s77, s99\n"
        "s_mov_b32 s77, s94\n"
        "s_and_b32 s94, s77, 2122219134\n"
        "s_cmp_lg_u32 s94, s98\n"
        "s_cbranch_scc1 L_sp26%=\n"
        "L_sp26b%=:\n"
        "ds_read2st64_b64 v[60:63], v52 offset0:0 offset1:0\n"
        "ds_read2st64_b64 v[64:67], v53 offset0:0 offset1:0\n"
        "ds_read2st64_b64 v[68:71], v54 offset0:0 offset1:0\n"
        "v_mfma_f32_16x16x32_bf16 v[32:35], v[152:155], v[48:51], v[32:35]\n"
        "ds_read2st64_b64 v[72:75], v55 offset0:0 offset1:0\n"
        "s_mov_b32 m0, s72\n"
        "v_cvt_pk_bf16_f32 v96, v24, v25\n"
        "v_mfma_f32_16x16x32_bf16 v[36:39], v[40:43], v[168:171], 0\n"
        "buffer_load_dwordx4 v78, s[28:31], s56 offen lds\n"
        "v_cvt_pk_bf16_f32 v97, v26, v27\n"
        "v_cvt_pk_bf16_f32 v112, v28, v29\n"
        "v_mfma_f32_16x16x32_bf16 v[36:39], v[44:47], v[172:175], v[36:39]\n"
        "buffer_load_dwordx4 v79, s[28:31], s56 offen offset:1024 lds\n"
        "v_cvt_pk_bf16_f32 v113, v30, v31\n"
        "v_mfma_f32_16x16x32_bf16 v[36:39], v[48:51], v[176:179], v[36:39]\n"
        "buffer_load_dwordx4 v80, s[28:31], s56 offen offset:2048 lds\n"
        "v_cvt_pk_bf16_f32 v114, v34, v35\n"
        "v_mul_f32 v32, s78, v32\n"
        "v_mfma_f32_16x16x32_bf16 v[16:19], v[40:43], v[180:183], v[12:15]\n"
        "s_sub_i32 s72, s73, s72\n"
        "v_mul_f32 v33, s78, v33\n"
        "v_cvt_pk_bf16_f32 v130, v32, v33\n"
        "v_mfma_f32_16x16x32_bf16 v[16:19], v[44:47], v[184:187], v[16:19]\n"
        "v_mfma_f32_16x16x32_bf16 v[16:19], v[48:51], v[188:191], v[16:19]\n"
        "v_mfma_f32_16x16x32_bf16 v[32:35], v[132:135], v[40:43], 0\n"
        "v_cvt_pk_bf16_f32 v90, v36, v37\n"
        "v_cvt_pk_bf16_f32 v91, v38, v39\n"
        "v_mfma_f32_16x16x32_bf16 v[0:3], v[100:103], v[116:119], v[192:195]\n"
        "v_mfma_f32_16x16x32_bf16 v[32:35], v[136:139], v[44:47], v[32:35]\n"
        "v_mfma_f32_16x16x32_bf16 v[4:7], v[104:107], v[116:119], v[196:199]\n"
        "v_mfma_f32_16x16x32_bf16 v[32:35], v[140:143], v[48:51], v[32:35]\n"
        "v_mfma_f32_16x16x32_bf16 v[8:11], v[108:111], v[116:119], v[200:203]\n"
        "v_cvt_pk_bf16_f32 v98, v16, v17\n"
        "v_cvt_pk_bf16_f32 v99, v18, v19\n"
        "v_mfma_f32_16x16x32_bf16 v[12:15], v[112:115], v[116:119], v[204:207]\n"
        "s_waitcnt lgkmcnt(0)\n"
        "ds_read2st64_b64 v[40:43], v52 offset0:4 offset1:4\n"
        "ds_read2st64_b64 v[44:47], v53 offset0:4 offset1:4\n"
        "v_mfma_f32_16x16x32_bf16 v[0:3], v[56:59], v[60:63], v[0:3]\n"
        "ds_read2st64_b64 v[48:51], v54 offset0:4 offset1:4\n"
        "ds_read2st64_b64 v[60:63], v55 offset0:4 offset1:4\n"
        "v_mfma_f32_16x16x32_bf16 v[4:7], v[56:59], v[64:67], v[4:7]\n"
        "s_setprio 2\n"
        "s_cmp_eq_u32 s84, 0\n"
        "s_cbranch_scc1 L_p27%=\n"
        "s_setprio 0\n"
        "L_p27%=:\n"
        "v_mfma_f32_16x16x32_bf16 v[8:11], v[56:59], v[68:71], v[8:11]\n"
        "v_mfma_f32_16x16x32_bf16 v[12:15], v[56:59], v[72:75], v[12:15]\n"
        "v_exp_f32 v0, v0\n"
        "v_exp_f32 v1, v1\n"
        "v_exp_f32 v2, v2\n"
        "v_exp_f32 v3, v3\n"
        "v_mfma_f32_16x16x32_bf16 v[16:19], v[100:103], v[120:123], v[208:211]\n"
        "s_waitcnt lgkmcnt(2)\n"
        "v_mfma_f32_16x16x32_bf16 v[16:19], v[56:59], v[40:43], v[16:19]\n"
        "ds_read2st64_b64 v[64:67], v52 offset0:8 offset1:8\n"
        "v_exp_f32 v4, v4\n"
        "v_exp_f32 v5, v5\n"
        "v_mfma_f32_16x16x32_bf16 v[20:23], v[104:107], v[120:123], v[212:215]\n"
        "v_mfma_f32_16x16x32_bf16 v[20:23], v[56:59], v[44:47], v[20:23]\n"
        "v_exp_f32 v6, v6\n"
        "v_exp_f32 v7, v7\n"
        "v_cvt_pk_bf16_f32 v0, v0, v1\n"
        "v_cvt_pk_bf16_f32 v1, v2, v3\n"
        "v_cvt_pk_bf16_f32 v2, v4, v5\n"
        "v_cvt_pk_bf16_f32 v3, v6, v7\n"
        "v_mul_f32 v32, s78, v32\n"
        "v_mul_f32 v33, s78, v33\n"
        "v_mul_f32 v34, s78, v34\n"
        "v_mul_f32 v35, s78, v35\n"
        "v_cvt_pk_bf16_f32 v128, v32, v33\n"
        "v_cvt_pk_bf16_f32 v129, v34, v35\n"
        "v_mfma_f32_16x16x32_bf16 v[24:27], v[108:111], v[120:123], v[216:219]\n"
        "ds_read2st64_b64 v[68:71], v53 offset0:8 offset1:8\n"
        "ds_read2st64_b64 v[72:75], v54 offset0:8 offset1:8\n"
        "s_waitcnt lgkmcnt(4)\n"
        "v_mfma_f32_16x16x32_bf16 v[24:27], v[56:59], v[48:51], v[24:27]\n"
        "v_mfma_f32_16x16x32_bf16 v[28:31], v[112:115], v[120:123], v[220:223]\n"
        "s_waitcnt lgkmcnt(3)\n"
        "v_mfma_f32_16x16x32_bf16 v[28:31], v[56:59], v[60:63], v[28:31]\n"
        "v_exp_f32 v8, v8\n"
        "v_mfma_f32_16x16x32_bf16 v[32:35], v[84:87], v[0:3], 0\n"
        "v_exp_f32 v9, v9\n"
        "v_mfma_f32_16x16x32_bf16 v[36:39], v[92:95], v[0:3], 0\n"
        "v_exp_f32 v10, v10\n"
        "v_exp_f32 v11, v11\n"
        "v_exp_f32 v12, v12\n"
        "v_exp_f32 v13, v13\n"
        "v_exp_f32 v14, v14\n"
        "v_exp_f32 v15, v15\n"
        "v_cvt_pk_bf16_f32 v8, v8, v9\n"
        "v_cvt_pk_bf16_f32 v9, v10, v11\n"
        "v_cvt_pk_bf16_f32 v10, v12, v13\n"
        "v_cvt_pk_bf16_f32 v11, v14, v15\n"
        "v_exp_f32 v16, v16\n"
        "v_exp_f32 v17, v17\n"
        "v_mfma_f32_16x16x32_bf16 v[32:35], v[88:91], v[8:11], v[32:35]\n"
        "ds_read2st64_b64 v[40:43], v55 offset0:8 offset1:8\n"
        "v_exp_f32 v18, v18\n"
        "v_mfma_f32_16x16x32_bf16 v[36:39], v[96:99], v[8:11], v[36:39]\n"
        "v_exp_f32 v19, v19\n"
        "v_mfma_f32_16x16x32_bf16 v[0:3], v[100:103], v[124:127], v[224:227]\n"
        "s_waitcnt lgkmcnt(3)\n"
        "v_mfma_f32_16x16x32_bf16 v[0:3], v[56:59], v[64:67], v[0:3]\n"
        "ds_read2st64_b64 v[44:47], v52 offset0:12 offset1:12\n"
        "v_exp_f32 v20, v20\n"
        "v_exp_f32 v21, v21\n"
        "v_mfma_f32_16x16x32_bf16 v[4:7], v[104:107], v[124:127], v[228:231]\n"
        "s_waitcnt lgkmcnt(3)\n"
        "v_mfma_f32_16x16x32_bf16 v[4:7], v[56:59], v[68:71], v[4:7]\n"
        "v_exp_f32 v22, v22\n"
        "v_exp_f32 v23, v23\n"
        "v_rcp_f32 v39, v38\n"
        "v_cvt_pk_bf16_f32 v16, v16, v17\n"
        "v_cvt_pk_bf16_f32 v17, v18, v19\n"
        "v_cvt_pk_bf16_f32 v18, v20, v21\n"
        "v_cvt_pk_bf16_f32 v19, v22, v23\n"
        "v_mul_f32 v32, v39, v32\n"
        "v_mul_f32 v33, v39, v33\n"
        "v_mul_f32 v34, v39, v34\n"
        "v_mul_f32 v35, v39, v35\n"
        "v_cvt_pk_bf16_f32 v32, v32, v33\n"
        "v_cvt_pk_bf16_f32 v33, v34, v35\n"
        "v_mfma_f32_16x16x32_bf16 v[8:11], v[108:111], v[124:127], v[232:235]\n"
        "ds_write_b64 v81, v[32:33] offset:18688\n"
        "s_waitcnt lgkmcnt(3)\n"
        "v_mfma_f32_16x16x32_bf16 v[8:11], v[56:59], v[72:75], v[8:11]\n"
        "ds_read2st64_b64 v[48:51], v53 offset0:12 offset1:12\n"
        "v_mul_f32 v36, v39, v36\n"
        "v_mul_f32 v37, v39, v37\n"
        "v_cvt_pk_bf16_f32 v36, v36, v37\n"
        "v_maximum3_f32 v76, v39, v38, v39\n"
        "v_mfma_f32_16x16x32_bf16 v[12:15], v[112:115], v[124:127], v[236:239]\n"
        "ds_write_b32 v82, v36 offset:18688\n"
        "ds_read2st64_b64 v[60:63], v54 offset0:12 offset1:12\n"
        "s_waitcnt lgkmcnt(5)\n"
        "v_mfma_f32_16x16x32_bf16 v[12:15], v[56:59], v[40:43], v[12:15]\n"
        "v_exp_f32 v24, v24\n"
        "v_mfma_f32_16x16x32_bf16 v[32:35], v[84:87], v[16:19], 0\n"
        "v_exp_f32 v25, v25\n"
        "v_mfma_f32_16x16x32_bf16 v[36:39], v[92:95], v[16:19], 0\n"
        "v_exp_f32 v26, v26\n"
        "v_exp_f32 v27, v27\n"
        "v_exp_f32 v28, v28\n"
        "v_exp_f32 v29, v29\n"
        "v_exp_f32 v30, v30\n"
        "v_exp_f32 v31, v31\n"
        "v_cvt_pk_bf16_f32 v24, v24, v25\n"
        "v_cvt_pk_bf16_f32 v25, v26, v27\n"
        "v_cvt_pk_bf16_f32 v26, v28, v29\n"
        "v_cvt_pk_bf16_f32 v27, v30, v31\n"
        "v_exp_f32 v0, v0\n"
        "v_exp_f32 v1, v1\n"
        "v_mfma_f32_16x16x32_bf16 v[32:35], v[88:91], v[24:27], v[32:35]\n"
        "ds_read2st64_b64 v[64:67], v55 offset0:12 offset1:12\n"
        "v_exp_f32 v2, v2\n"
        "v_mfma_f32_16x16x32_bf16 v[36:39], v[96:99], v[24:27], v[36:39]\n"
        "v_exp_f32 v3, v3\n"
        "v_mfma_f32_16x16x32_bf16 v[16:19], v[100:103], v[128:131], v[240:243]\n"
        "s_waitcnt lgkmcnt(5)\n"
        "v_mfma_f32_16x16x32_bf16 v[16:19], v[56:59], v[44:47], v[16:19]\n"
        "v_exp_f32 v4, v4\n"
        "v_exp_f32 v5, v5\n"
        "v_mfma_f32_16x16x32_bf16 v[20:23], v[104:107], v[128:131], v[244:247]\n"
        "s_waitcnt lgkmcnt(3)\n"
        "v_mfma_f32_16x16x32_bf16 v[20:23], v[56:59], v[48:51], v[20:23]\n"
        "v_exp_f32 v6, v6\n"
        "v_exp_f32 v7, v7\n"
        "v_rcp_f32 v39, v38\n"
        "v_cvt_pk_bf16_f32 v0, v0, v1\n"
        "v_cvt_pk_bf16_f32 v1, v2, v3\n"
        "v_cvt_pk_bf16_f32 v2, v4, v5\n"
        "v_cvt_pk_bf16_f32 v3, v6, v7\n"
        "v_mul_f32 v32, v39, v32\n"
        "v_mul_f32 v33, v39, v33\n"
        "v_mul_f32 v34, v39, v34\n"
        "v_mul_f32 v35, v39, v35\n"
        "v_cvt_pk_bf16_f32 v32, v32, v33\n"
        "v_cvt_pk_bf16_f32 v33, v34, v35\n"
        "v_mfma_f32_16x16x32_bf16 v[24:27], v[108:111], v[128:131], v[248:251]\n"
        "ds_write_b64 v81, v[32:33] offset:18944\n"
        "s_waitcnt lgkmcnt(2)\n"
        "v_mfma_f32_16x16x32_bf16 v[24:27], v[56:59], v[60:63], v[24:27]\n"
        "v_mul_f32 v36, v39, v36\n"
        "v_mul_f32 v37, v39, v37\n"
        "v_cvt_pk_bf16_f32 v36, v36, v37\n"
        "v_maximum3_f32 v76, v76, v38, v39\n"
        "v_mfma_f32_16x16x32_bf16 v[28:31], v[112:115], v[128:131], v[252:255]\n"
        "ds_write_b32 v82, v36 offset:18944\n"
        "s_waitcnt lgkmcnt(2)\n"
        "v_mfma_f32_16x16x32_bf16 v[28:31], v[56:59], v[64:67], v[28:31]\n"
        "v_exp_f32 v8, v8\n"
        "v_mfma_f32_16x16x32_bf16 v[32:35], v[84:87], v[0:3], 0\n"
        "v_exp_f32 v9, v9\n"
        "v_mfma_f32_16x16x32_bf16 v[36:39], v[92:95], v[0:3], 0\n"
        "v_exp_f32 v10, v10\n"
        "v_exp_f32 v11, v11\n"
        "v_exp_f32 v12, v12\n"
        "v_exp_f32 v13, v13\n"
        "v_exp_f32 v14, v14\n"
        "v_exp_f32 v15, v15\n"
        "v_cvt_pk_bf16_f32 v8, v8, v9\n"
        "v_cvt_pk_bf16_f32 v9, v10, v11\n"
        "v_cvt_pk_bf16_f32 v10, v12, v13\n"
        "v_cvt_pk_bf16_f32 v11, v14, v15\n"
        "v_exp_f32 v16, v16\n"
        "v_exp_f32 v17, v17\n"
        "v_mfma_f32_16x16x32_bf16 v[32:35], v[88:91], v[8:11], v[32:35]\n"
        "v_exp_f32 v18, v18\n"
        "v_mfma_f32_16x16x32_bf16 v[36:39], v[96:99], v[8:11], v[36:39]\n"
        "v_exp_f32 v19, v19\n"
        "v_exp_f32 v20, v20\n"
        "v_exp_f32 v21, v21\n"
        "v_exp_f32 v22, v22\n"
        "v_exp_f32 v23, v23\n"
        "v_cvt_pk_bf16_f32 v16, v16, v17\n"
        "v_cvt_pk_bf16_f32 v17, v18, v19\n"
        "s_nop 0\n"
        "v_rcp_f32 v39, v38\n"
        "v_cvt_pk_bf16_f32 v18, v20, v21\n"
        "v_cvt_pk_bf16_f32 v19, v22, v23\n"
        "v_mul_f32 v32, v39, v32\n"
        "v_mul_f32 v33, v39, v33\n"
        "v_mul_f32 v34, v39, v34\n"
        "v_mul_f32 v35, v39, v35\n"
        "v_cvt_pk_bf16_f32 v32, v32, v33\n"
        "v_cvt_pk_bf16_f32 v33, v34, v35\n"
        "ds_write_b64 v81, v[32:33] offset:19200\n"
        "v_mul_f32 v36, v39, v36\n"
        "v_mul_f32 v37, v39, v37\n"
        "v_cvt_pk_bf16_f32 v36, v36, v37\n"
        "v_maximum3_f32 v76, v76, v38, v39\n"
        "v_mov_b32 v63, s49\n"
        "v_mov_b32 v62, s55\n"
        "v_exp_f32 v24, v24\n"
        "v_mfma_f32_16x16x32_bf16 v[32:35], v[84:87], v[16:19], 0\n"
        "ds_write_b32 v82, v36 offset:19200\n"
        "v_exp_f32 v25, v25\n"
        "v_mfma_f32_16x16x32_bf16 v[36:39], v[92:95], v[16:19], 0\n"
        "ds_write_b8 v63, v62\n"
        "v_exp_f32 v26, v26\n"
        "v_exp_f32 v27, v27\n"
        "v_exp_f32 v28, v28\n"
        "v_exp_f32 v29, v29\n"
        "v_exp_f32 v30, v30\n"
        "v_exp_f32 v31, v31\n"
        "v_cvt_pk_bf16_f32 v24, v24, v25\n"
        "v_cvt_pk_bf16_f32 v25, v26, v27\n"
        "v_cvt_pk_bf16_f32 v26, v28, v29\n"
        "v_cvt_pk_bf16_f32 v27, v30, v31\n"
        "s_nop 1\n"
        "v_mfma_f32_16x16x32_bf16 v[32:35], v[88:91], v[24:27], v[32:35]\n"
        "ds_read_b128 v[52:55], v115 offset:18688\n"
        "ds_read_b128 v[56:59], v115 offset:22784\n"
        "ds_read_b128 v[60:63], v115 offset:26880\n"
        "ds_read_b128 v[8:11], v107 offset:2048\n"
        "v_mfma_f32_16x16x32_bf16 v[36:39], v[96:99], v[24:27], v[36:39]\n"
        "ds_read_b128 v[0:3], v107\n"
        "ds_read_b128 v[4:7], v107 offset:1024\n"
        "s_waitcnt lgkmcnt(3)\n"
        "s_waitcnt lgkmcnt(0)\n"
        "v_subrev_u32 v115, s75, v115\n"
        "v_mfma_f32_16x16x32_bf16 v[16:19], v[52:55], v[0:3], 0\n"
        "ds_read_b128 v[12:15], v107 offset:3120\n"
        "ds_read_b128 v[64:67], v107 offset:4144\n"
        "ds_read_b128 v[68:71], v107 offset:5168\n"
        "v_mfma_f32_16x16x32_bf16 v[16:19], v[56:59], v[4:7], v[16:19]\n"
        "ds_read_b128 v[72:75], v107 offset:6240\n"
        "ds_read_b128 v[40:43], v107 offset:7264\n"
        "ds_read_b128 v[44:47], v107 offset:8288\n"
        "v_mfma_f32_16x16x32_bf16 v[16:19], v[60:63], v[8:11], v[16:19]\n"
        "ds_read_b128 v[0:3], v107 offset:9360\n"
        "ds_read_b128 v[4:7], v107 offset:10384\n"
        "ds_read_b128 v[8:11], v107 offset:11408\n"
        "s_waitcnt lgkmcnt(6)\n"
        "v_mfma_f32_16x16x32_bf16 v[20:23], v[52:55], v[12:15], 0\n"
        "ds_read_b128 v[12:15], v107 offset:12480\n"
        "v_mfma_f32_16x16x32_bf16 v[20:23], v[56:59], v[64:67], v[20:23]\n"
        "ds_read_b128 v[64:67], v107 offset:13504\n"
        "v_mfma_f32_16x16x32_bf16 v[20:23], v[60:63], v[68:71], v[20:23]\n"
        "ds_read_b128 v[68:71], v107 offset:14528\n"
        "s_waitcnt lgkmcnt(6)\n"
        "v_mfma_f32_16x16x32_bf16 v[24:27], v[52:55], v[72:75], 0\n"
        "ds_read_b128 v[72:75], v107 offset:15600\n"
        "v_mfma_f32_16x16x32_bf16 v[24:27], v[56:59], v[40:43], v[24:27]\n"
        "ds_read_b128 v[40:43], v107 offset:16624\n"
        "v_mfma_f32_16x16x32_bf16 v[24:27], v[60:63], v[44:47], v[24:27]\n"
        "ds_read_b128 v[44:47], v107 offset:17648\n"
        "s_waitcnt lgkmcnt(6)\n"
        "v_mfma_f32_16x16x32_bf16 v[28:31], v[52:55], v[0:3], 0\n"
        "v_mfma_f32_16x16x32_bf16 v[28:31], v[56:59], v[4:7], v[28:31]\n"
        "v_mfma_f32_16x16x32_bf16 v[28:31], v[60:63], v[8:11], v[28:31]\n"
        "s_waitcnt lgkmcnt(3)\n"
        "v_cvt_pk_bf16_f32 v116, v16, v20\n"
        "v_cvt_pk_bf16_f32 v120, v17, v21\n"
        "v_cvt_pk_bf16_f32 v124, v18, v22\n"
        "v_cvt_pk_bf16_f32 v128, v19, v23\n"
        "v_mfma_f32_16x16x32_bf16 v[48:51], v[52:55], v[12:15], 0\n"
        "v_mfma_f32_16x16x32_bf16 v[48:51], v[56:59], v[64:67], v[48:51]\n"
        "v_mfma_f32_16x16x32_bf16 v[48:51], v[60:63], v[68:71], v[48:51]\n"
        "s_waitcnt lgkmcnt(0)\n"
        "v_mfma_f32_16x16x32_bf16 v[16:19], v[52:55], v[72:75], 0\n"
        "v_mfma_f32_16x16x32_bf16 v[16:19], v[56:59], v[40:43], v[16:19]\n"
        "s_xor_b32 s81, s81, 8\n"
        "s_xor_b32 s82, s82, 8\n"
        "v_mfma_f32_16x16x32_bf16 v[16:19], v[60:63], v[44:47], v[16:19]\n"
        "s_mov_b32 s48, s98\n"
        "s_and_b32 s76, s77, 16843009\n"
        "s_add_i32 s98, s98, 33686018\n"
        "s_add_i32 s64, s64, 1\n"
        "s_cmp_lt_u32 s64, 8\n"
        "v_cvt_pk_bf16_f32 v117, v24, v28\n"
        "v_cvt_pk_bf16_f32 v121, v25, v29\n"
        "v_cvt_pk_bf16_f32 v125, v26, v30\n"
        "v_cvt_pk_bf16_f32 v129, v27, v31\n"
        "v_cvt_pk_bf16_f32 v118, v48, v16\n"
        "buffer_store_dwordx3 v[116:118], v111, s[40:43], s58 offen sc1\n"
        "v_cvt_pk_bf16_f32 v122, v49, v17\n"
        "buffer_store_dwordx3 v[120:122], v111, s[40:43], s59 offen sc1\n"
        "v_cvt_pk_bf16_f32 v126, v50, v18\n"
        "buffer_store_dwordx3 v[124:126], v111, s[44:47], s58 offen sc1\n"
        "v_cvt_pk_bf16_f32 v130, v51, v19\n"
        "buffer_store_dwordx3 v[128:130], v111, s[44:47], s59 offen sc1\n"
        "s_mov_b32 s58, 1073741824\n"
        "s_mov_b32 s59, 1073741824\n"
        "ds_read_b128 v[0:3], v103 offset:43264\n"
        "ds_read_b128 v[4:7], v103 offset:43328\n"
        "ds_read_b128 v[8:11], v103 offset:43392\n"
        "s_mov_b64 s[34:35], s[58:59]\n"
        "s_mov_b64 s[58:59], s[60:61]\n"
        "s_mov_b64 s[60:61], s[62:63]\n"
        "s_mov_b64 s[62:63], s[56:57]\n"
        "v_add_f32 v12, v76, v38\n"
        "v_mov_b32 v13, 2139095040\n"
        "v_cmp_u_f32 vcc, v12, v12\n"
        "s_nop 1\n"
        "v_cndmask_b32 v76, v76, v13, vcc\n"
        "s_waitcnt lgkmcnt(0)\n"
        "s_branch L_back%=\n"
        "L_redo%=:\n"
        "s_waitcnt vmcnt(0) lgkmcnt(0)\n"
        "s_nop 7\n"
        "buffer_store_dwordx3 v[116:118], v111, s[40:43], s34 offen sc1\n"
        "buffer_store_dwordx3 v[120:122], v111, s[40:43], s35 offen sc1\n"
        "buffer_store_dwordx3 v[124:126], v111, s[44:47], s34 offen sc1\n"
        "buffer_store_dwordx3 v[128:130], v111, s[44:47], s35 offen sc1\n"
        "s_mov_b32 s34, 1073741824\n"
        "s_mov_b32 s35, 1073741824\n"
        "s_add_i32 s96, s64, -1\n"
        "s_lshl3_add_u32 s96, s96, s85\n"
        "s_and_b32 s96, s96, 63\n"
        "s_add_i32 s95, s68, s96\n"
        "s_and_b32 s95, s95, 63\n"
        "v_lshrrev_b32 v76, 4, v107\n"
        "v_and_b32 v77, 7, v76\n"
        "v_mul_u32_u24 v77, 192, v77\n"
        "v_bfe_u32 v16, v76, 3, 1\n"
        "v_mad_u32_u24 v77, v16, s83, v77\n"
        "v_lshrrev_b32 v16, 4, v76\n"
        "v_lshl_add_u32 v77, v16, 4, v77\n"
        "s_cmp_eq_u32 s95, 63\n"
        "s_cselect_b32 s92, 0, s83\n"
        "v_and_b32 v76, 4, v76\n"
        "v_cmp_ne_u32 vcc, 0, v76\n"
        "v_mov_b32 v16, s83\n"
        "v_mov_b32 v17, s92\n"
        "v_cndmask_b32 v16, v16, v17, vcc\n"
        "v_add_u32 v77, v77, v16\n"
        "s_mul_i32 s93, s95, 1536\n"
        "s_addk_i32 s93, 768\n"
        "s_lshl3_add_u32 s94, s96, s67\n"
        "s_lshl_b32 s92, %[wid], 1\n"
        "s_sub_i32 s94, s94, s92\n"
        "s_add_i32 s92, s94, 0\n"
        "s_and_b32 s92, s92, 511\n"
        "s_mul_i32 s92, s92, s83\n"
        "s_add_i32 s92, s92, s93\n"
        "buffer_load_dwordx4 v[0:3], v77, s[28:31], s92 offen\n"
        "buffer_load_dwordx4 v[4:7], v77, s[28:31], s92 offen offset:64\n"
        "buffer_load_dwordx4 v[8:11], v77, s[28:31], s92 offen offset:128\n"
        "s_waitcnt vmcnt(2)\n"
        "v_mfma_f32_16x16x32_bf16 v[16:19], v[132:135], v[0:3], 0\n"
        "s_waitcnt vmcnt(1)\n"
        "v_mfma_f32_16x16x32_bf16 v[16:19], v[136:139], v[4:7], v[16:19]\n"
        "s_waitcnt vmcnt(0)\n"
        "v_mfma_f32_16x16x32_bf16 v[16:19], v[140:143], v[8:11], v[16:19]\n"
        "v_mfma_f32_16x16x32_bf16 v[20:23], v[144:147], v[0:3], 0\n"
        "v_mfma_f32_16x16x32_bf16 v[20:23], v[148:151], v[4:7], v[20:23]\n"
        "v_mfma_f32_16x16x32_bf16 v[20:23], v[152:155], v[8:11], v[20:23]\n"
        "s_nop 4\n"
        "v_mul_f32 v16, s78, v16\n"
        "v_mul_f32 v17, s78, v17\n"
        "v_mul_f32 v18, s78, v18\n"
        "v_mul_f32 v19, s78, v19\n"
        "v_cvt_pk_bf16_f32 v116, v16, v17\n"
        "v_cvt_pk_bf16_f32 v117, v18, v19\n"
        "v_cvt_pk_bf16_f32 v102, v22, v23\n"
        "v_mul_f32 v20, s78, v20\n"
        "v_mul_f32 v21, s78, v21\n"
        "v_cvt_pk_bf16_f32 v118, v20, v21\n"
        "s_add_i32 s92, s94, 2\n"
        "s_and_b32 s92, s92, 511\n"
        "s_mul_i32 s92, s92, s83\n"
        "s_add_i32 s92, s92, s93\n"
        "buffer_load_dwordx4 v[40:43], v77, s[28:31], s92 offen\n"
        "buffer_load_dwordx4 v[44:47], v77, s[28:31], s92 offen offset:64\n"
        "buffer_load_dwordx4 v[48:51], v77, s[28:31], s92 offen offset:128\n"
        "s_waitcnt vmcnt(2)\n"
        "v_mfma_f32_16x16x32_bf16 v[16:19], v[132:135], v[40:43], 0\n"
        "s_waitcnt vmcnt(1)\n"
        "v_mfma_f32_16x16x32_bf16 v[16:19], v[136:139], v[44:47], v[16:19]\n"
        "s_waitcnt vmcnt(0)\n"
        "v_mfma_f32_16x16x32_bf16 v[16:19], v[140:143], v[48:51], v[16:19]\n"
        "v_mfma_f32_16x16x32_bf16 v[20:23], v[144:147], v[40:43], 0\n"
        "v_mfma_f32_16x16x32_bf16 v[20:23], v[148:151], v[44:47], v[20:23]\n"
        "v_mfma_f32_16x16x32_bf16 v[20:23], v[152:155], v[48:51], v[20:23]\n"
        "s_nop 4\n"
        "v_mul_f32 v16, s78, v16\n"
        "v_mul_f32 v17, s78, v17\n"
        "v_mul_f32 v18, s78, v18\n"
        "v_mul_f32 v19, s78, v19\n"
        "v_cvt_pk_bf16_f32 v120, v16, v17\n"
        "v_cvt_pk_bf16_f32 v121, v18, v19\n"
        "v_cvt_pk_bf16_f32 v106, v22, v23\n"
        "v_mul_f32 v20, s78, v20\n"
        "v_mul_f32 v21, s78, v21\n"
        "v_cvt_pk_bf16_f32 v122, v20, v21\n"
        "s_add_i32 s92, s94, 4\n"
        "s_and_b32 s92, s92, 511\n"
        "s_mul_i32 s92, s92, s83\n"
        "s_add_i32 s92, s92, s93\n"
        "buffer_load_dwordx4 v[0:3], v77, s[28:31], s92 offen\n"
        "buffer_load_dwordx4 v[4:7], v77, s[28:31], s92 offen offset:64\n"
        "buffer_load_dwordx4 v[8:11], v77, s[28:31], s92 offen offset:128\n"
        "s_waitcnt vmcnt(2)\n"
        "v_mfma_f32_16x16x32_bf16 v[16:19], v[132:135], v[0:3], 0\n"
        "s_waitcnt vmcnt(1)\n"
        "v_mfma_f32_16x16x32_bf16 v[16:19], v[136:139], v[4:7], v[16:19]\n"
        "s_waitcnt vmcnt(0)\n"
        "v_mfma_f32_16x16x32_bf16 v[16:19], v[140:143], v[8:11], v[16:19]\n"
        "v_mfma_f32_16x16x32_bf16 v[20:23], v[144:147], v[0:3], 0\n"
        "v_mfma_f32_16x16x32_bf16 v[20:23], v[148:151], v[4:7], v[20:23]\n"
        "v_mfma_f32_16x16x32_bf16 v[20:23], v[152:155], v[8:11], v[20:23]\n"
        "s_nop 4\n"
        "v_mul_f32 v16, s78, v16\n"
        "v_mul_f32 v17, s78, v17\n"
        "v_mul_f32 v18, s78, v18\n"
        "v_mul_f32 v19, s78, v19\n"
        "v_cvt_pk_bf16_f32 v124, v16, v17\n"
        "v_cvt_pk_bf16_f32 v125, v18, v19\n"
        "v_cvt_pk_bf16_f32 v110, v22, v23\n"
        "v_mul_f32 v20, s78, v20\n"
        "v_mul_f32 v21, s78, v21\n"
        "v_cvt_pk_bf16_f32 v126, v20, v21\n"
        "s_add_i32 s92, s94, 6\n"
        "s_and_b32 s92, s92, 511\n"
        "s_mul_i32 s92, s92, s83\n"
        "s_add_i32 s92, s92, s93\n"
        "buffer_load_dwordx4 v[40:43], v77, s[28:31], s92 offen\n"
        "buffer_load_dwordx4 v[44:47], v77, s[28:31], s92 offen offset:64\n"
        "buffer_load_dwordx4 v[48:51], v77, s[28:31], s92 offen offset:128\n"
        "s_waitcnt vmcnt(2)\n"
        "v_mfma_f32_16x16x32_bf16 v[16:19], v[132:135], v[40:43], 0\n"
        "s_waitcnt vmcnt(1)\n"
        "v_mfma_f32_16x16x32_bf16 v[16:19], v[136:139], v[44:47], v[16:19]\n"
        "s_waitcnt vmcnt(0)\n"
        "v_mfma_f32_16x16x32_bf16 v[16:19], v[140:143], v[48:51], v[16:19]\n"
        "v_mfma_f32_16x16x32_bf16 v[20:23], v[144:147], v[40:43], 0\n"
        "v_mfma_f32_16x16x32_bf16 v[20:23], v[148:151], v[44:47], v[20:23]\n"
        "v_mfma_f32_16x16x32_bf16 v[20:23], v[152:155], v[48:51], v[20:23]\n"
        "s_nop 4\n"
        "v_mul_f32 v16, s78, v16\n"
        "v_mul_f32 v17, s78, v17\n"
        "v_mul_f32 v18, s78, v18\n"
        "v_mul_f32 v19, s78, v19\n"
        "v_cvt_pk_bf16_f32 v128, v16, v17\n"
        "v_cvt_pk_bf16_f32 v129, v18, v19\n"
        "v_cvt_pk_bf16_f32 v114, v22, v23\n"
        "v_mul_f32 v20, s78, v20\n"
        "v_mul_f32 v21, s78, v21\n"
        "v_cvt_pk_bf16_f32 v130, v20, v21\n"
        "s_lshl_b32 s97, s96, 19\n"
        "s_lshl_b32 s93, s95, 13\n"
        "s_add_i32 s97, s97, s93\n"
        "s_add_i32 s97, s97, s69\n"
        "s_and_b32 s97, s97, 4294959104\n"
        "v_lshrrev_b32 v77, 8, v107\n"
        "v_and_b32 v76, 240, v107\n"
        "v_lshlrev_b32 v76, 3, v76\n"
        "v_lshl_add_u32 v76, v77, 3, v76\n"
        "buffer_load_dwordx2 v[40:41], v76, s[36:39], s97 offen\n"
        "buffer_load_dwordx2 v[42:43], v76, s[36:39], s97 offen offset:32\n"
        "buffer_load_dwordx2 v[44:45], v76, s[36:39], s97 offen offset:64\n"
        "buffer_load_dwordx2 v[46:47], v76, s[36:39], s97 offen offset:96\n"
        "buffer_load_dwordx2 v[48:49], v76, s[36:39], s97 offen offset:2048\n"
        "buffer_load_dwordx2 v[50:51], v76, s[36:39], s97 offen offset:2080\n"
        "buffer_load_dwordx2 v[52:53], v76, s[36:39], s97 offen offset:2112\n"
        "buffer_load_dwordx2 v[54:55], v76, s[36:39], s97 offen offset:2144\n"
        "s_addk_i32 s97, 4096\n"
        "buffer_load_dwordx2 v[56:57], v76, s[36:39], s97 offen\n"
        "buffer_load_dwordx2 v[58:59], v76, s[36:39], s97 offen offset:32\n"
        "buffer_load_dwordx2 v[60:61], v76, s[36:39], s97 offen offset:64\n"
        "buffer_load_dwordx2 v[62:63], v76, s[36:39], s97 offen offset:96\n"
        "buffer_load_dwordx2 v[64:65], v76, s[36:39], s97 offen offset:2048\n"
        "buffer_load_dwordx2 v[66:67], v76, s[36:39], s97 offen offset:2080\n"
        "buffer_load_dwordx2 v[68:69], v76, s[36:39], s97 offen offset:2112\n"
        "buffer_load_dwordx2 v[70:71], v76, s[36:39], s97 offen offset:2144\n"
        "v_mfma_f32_16x16x32_bf16 v[0:3], v[100:103], v[116:119], v[192:195]\n"
        "v_mfma_f32_16x16x32_bf16 v[4:7], v[104:107], v[116:119], v[196:199]\n"
        "v_mfma_f32_16x16x32_bf16 v[8:11], v[108:111], v[116:119], v[200:203]\n"
        "v_mfma_f32_16x16x32_bf16 v[12:15], v[112:115], v[116:119], v[204:207]\n"
        "s_waitcnt vmcnt(15)\n"
        "v_lshlrev_b32 v72, 16, v40\n"
        "v_and_b32 v73, 4294901760, v40\n"
        "v_lshlrev_b32 v74, 16, v41\n"
        "v_and_b32 v75, 4294901760, v41\n"
        "v_fmac_f32 v0, s80, v72\n"
        "v_fmac_f32 v1, s80, v73\n"
        "v_fmac_f32 v2, s80, v74\n"
        "v_fmac_f32 v3, s80, v75\n"
        "s_waitcnt vmcnt(14)\n"
        "v_lshlrev_b32 v72, 16, v42\n"
        "v_and_b32 v73, 4294901760, v42\n"
        "v_lshlrev_b32 v74, 16, v43\n"
        "v_and_b32 v75, 4294901760, v43\n"
        "v_fmac_f32 v4, s80, v72\n"
        "v_fmac_f32 v5, s80, v73\n"
        "v_fmac_f32 v6, s80, v74\n"
        "v_fmac_f32 v7, s80, v75\n"
        "s_waitcnt vmcnt(13)\n"
        "v_lshlrev_b32 v72, 16, v44\n"
        "v_and_b32 v73, 4294901760, v44\n"
        "v_lshlrev_b32 v74, 16, v45\n"
        "v_and_b32 v75, 4294901760, v45\n"
        "v_fmac_f32 v8, s80, v72\n"
        "v_fmac_f32 v9, s80, v73\n"
        "v_fmac_f32 v10, s80, v74\n"
        "v_fmac_f32 v11, s80, v75\n"
        "s_waitcnt vmcnt(12)\n"
        "v_lshlrev_b32 v72, 16, v46\n"
        "v_and_b32 v73, 4294901760, v46\n"
        "v_lshlrev_b32 v74, 16, v47\n"
        "v_and_b32 v75, 4294901760, v47\n"
        "v_fmac_f32 v12, s80, v72\n"
        "v_fmac_f32 v13, s80, v73\n"
        "v_fmac_f32 v14, s80, v74\n"
        "v_fmac_f32 v15, s80, v75\n"
        "v_max3_f32 v16, v0, v1, v2\n"
        "v_max3_f32 v16, v16, v3, v4\n"
        "v_max3_f32 v16, v16, v5, v6\n"
        "v_max3_f32 v16, v16, v7, v8\n"
        "v_max3_f32 v16, v16, v9, v10\n"
        "v_max3_f32 v16, v16, v11, v12\n"
        "v_max3_f32 v16, v16, v13, v14\n"
        "v_max_f32 v16, v16, v15\n"
        "v_mov_b32 v17, v16\n"
        "s_nop 1\n"
        "v_permlane16_swap_b32 v16, v17\n"
        "v_max_f32 v16, v16, v17\n"
        "v_mov_b32 v17, v16\n"
        "s_nop 1\n"
        "v_permlane32_swap_b32 v16, v17\n"
        "v_max_f32 v16, v16, v17\n"
        "v_sub_f32 v0, v0, v16\n"
        "v_sub_f32 v1, v1, v16\n"
        "v_sub_f32 v2, v2, v16\n"
        "v_sub_f32 v3, v3, v16\n"
        "v_sub_f32 v4, v4, v16\n"
        "v_sub_f32 v5, v5, v16\n"
        "v_sub_f32 v6, v6, v16\n"
        "v_sub_f32 v7, v7, v16\n"
        "v_sub_f32 v8, v8, v16\n"
        "v_sub_f32 v9, v9, v16\n"
        "v_sub_f32 v10, v10, v16\n"
        "v_sub_f32 v11, v11, v16\n"
        "v_sub_f32 v12, v12, v16\n"
        "v_sub_f32 v13, v13, v16\n"
        "v_sub_f32 v14, v14, v16\n"
        "v_sub_f32 v15, v15, v16\n"
        "v_exp_f32 v0, v0\n"
        "v_exp_f32 v1, v1\n"
        "v_exp_f32 v2, v2\n"
        "v_exp_f32 v3, v3\n"
        "v_exp_f32 v4, v4\n"
        "v_exp_f32 v5, v5\n"
        "v_exp_f32 v6, v6\n"
        "v_exp_f32 v7, v7\n"
        "v_exp_f32 v8, v8\n"
        "v_exp_f32 v9, v9\n"
        "v_exp_f32 v10, v10\n"
        "v_exp_f32 v11, v11\n"
        "v_exp_f32 v12, v12\n"
        "v_exp_f32 v13, v13\n"
        "v_exp_f32 v14, v14\n"
        "v_exp_f32 v15, v15\n"
        "v_cvt_pk_bf16_f32 v0, v0, v1\n"
        "v_cvt_pk_bf16_f32 v1, v2, v3\n"
        "v_cvt_pk_bf16_f32 v2, v4, v5\n"
        "v_cvt_pk_bf16_f32 v3, v6, v7\n"
        "v_cvt_pk_bf16_f32 v8, v8, v9\n"
        "v_cvt_pk_bf16_f32 v9, v10, v11\n"
        "v_cvt_pk_bf16_f32 v10, v12, v13\n"
        "v_cvt_pk_bf16_f32 v11, v14, v15\n"
        "v_mfma_f32_16x16x32_bf16 v[32:35], v[84:87], v[0:3], 0\n"
        "v_mfma_f32_16x16x32_bf16 v[36:39], v[92:95], v[0:3], 0\n"
        "v_mfma_f32_16x16x32_bf16 v[32:35], v[88:91], v[8:11], v[32:35]\n"
        "v_mfma_f32_16x16x32_bf16 v[36:39], v[96:99], v[8:11], v[36:39]\n"
        "s_nop 7\n"
        "v_rcp_f32 v39, v38\n"
        "s_nop 0\n"
        "v_mul_f32 v32, v39, v32\n"
        "v_mul_f32 v33, v39, v33\n"
        "v_mul_f32 v34, v39, v34\n"
        "v_mul_f32 v35, v39, v35\n"
        "v_cvt_pk_bf16_f32 v32, v32, v33\n"
        "v_cvt_pk_bf16_f32 v33, v34, v35\n"
        "ds_write_b64 v81, v[32:33] offset:18688\n"
        "v_mul_f32 v36, v39, v36\n"
        "v_mul_f32 v37, v39, v37\n"
        "v_cvt_pk_bf16_f32 v36, v36, v37\n"
        "ds_write_b32 v82, v36 offset:18688\n"
        "v_mfma_f32_16x16x32_bf16 v[0:3], v[100:103], v[120:123], v[208:211]\n"
        "v_mfma_f32_16x16x32_bf16 v[4:7], v[104:107], v[120:123], v[212:215]\n"
        "v_mfma_f32_16x16x32_bf16 v[8:11], v[108:111], v[120:123], v[216:219]\n"
        "v_mfma_f32_16x16x32_bf16 v[12:15], v[112:115], v[120:123], v[220:223]\n"
        "s_waitcnt vmcnt(11)\n"
        "v_lshlrev_b32 v72, 16, v48\n"
        "v_and_b32 v73, 4294901760, v48\n"
        "v_lshlrev_b32 v74, 16, v49\n"
        "v_and_b32 v75, 4294901760, v49\n"
        "v_fmac_f32 v0, s80, v72\n"
        "v_fmac_f32 v1, s80, v73\n"
        "v_fmac_f32 v2, s80, v74\n"
        "v_fmac_f32 v3, s80, v75\n"
        "s_waitcnt vmcnt(10)\n"
        "v_lshlrev_b32 v72, 16, v50\n"
        "v_and_b32 v73, 4294901760, v50\n"
        "v_lshlrev_b32 v74, 16, v51\n"
        "v_and_b32 v75, 4294901760, v51\n"
        "v_fmac_f32 v4, s80, v72\n"
        "v_fmac_f32 v5, s80, v73\n"
        "v_fmac_f32 v6, s80, v74\n"
        "v_fmac_f32 v7, s80, v75\n"
        "s_waitcnt vmcnt(9)\n"
        "v_lshlrev_b32 v72, 16, v52\n"
        "v_and_b32 v73, 4294901760, v52\n"
        "v_lshlrev_b32 v74, 16, v53\n"
        "v_and_b32 v75, 4294901760, v53\n"
        "v_fmac_f32 v8, s80, v72\n"
        "v_fmac_f32 v9, s80, v73\n"
        "v_fmac_f32 v10, s80, v74\n"
        "v_fmac_f32 v11, s80, v75\n"
        "s_waitcnt vmcnt(8)\n"
        "v_lshlrev_b32 v72, 16, v54\n"
        "v_and_b32 v73, 4294901760, v54\n"
        "v_lshlrev_b32 v74, 16, v55\n"
        "v_and_b32 v75, 4294901760, v55\n"
        "v_fmac_f32 v12, s80, v72\n"
        "v_fmac_f32 v13, s80, v73\n"
        "v_fmac_f32 v14, s80, v74\n"
        "v_fmac_f32 v15, s80, v75\n"
        "v_max3_f32 v16, v0, v1, v2\n"
        "v_max3_f32 v16, v16, v3, v4\n"
        "v_max3_f32 v16, v16, v5, v6\n"
        "v_max3_f32 v16, v16, v7, v8\n"
        "v_max3_f32 v16, v16, v9, v10\n"
        "v_max3_f32 v16, v16, v11, v12\n"
        "v_max3_f32 v16, v16, v13, v14\n"
        "v_max_f32 v16, v16, v15\n"
        "v_mov_b32 v17, v16\n"
        "s_nop 1\n"
        "v_permlane16_swap_b32 v16, v17\n"
        "v_max_f32 v16, v16, v17\n"
        "v_mov_b32 v17, v16\n"
        "s_nop 1\n"
        "v_permlane32_swap_b32 v16, v17\n"
        "v_max_f32 v16, v16, v17\n"
        "v_sub_f32 v0, v0, v16\n"
        "v_sub_f32 v1, v1, v16\n"
        "v_sub_f32 v2, v2, v16\n"
        "v_sub_f32 v3, v3, v16\n"
        "v_sub_f32 v4, v4, v16\n"
        "v_sub_f32 v5, v5, v16\n"
        "v_sub_f32 v6, v6, v16\n"
        "v_sub_f32 v7, v7, v16\n"
        "v_sub_f32 v8, v8, v16\n"
        "v_sub_f32 v9, v9, v16\n"
        "v_sub_f32 v10, v10, v16\n"
        "v_sub_f32 v11, v11, v16\n"
        "v_sub_f32 v12, v12, v16\n"
        "v_sub_f32 v13, v13, v16\n"
        "v_sub_f32 v14, v14, v16\n"
        "v_sub_f32 v15, v15, v16\n"
        "v_exp_f32 v0, v0\n"
        "v_exp_f32 v1, v1\n"
        "v_exp_f32 v2, v2\n"
        "v_exp_f32 v3, v3\n"
        "v_exp_f32 v4, v4\n"
        "v_exp_f32 v5, v5\n"
        "v_exp_f32 v6, v6\n"
        "v_exp_f32 v7, v7\n"
        "v_exp_f32 v8, v8\n"
        "v_exp_f32 v9, v9\n"
        "v_exp_f32 v10, v10\n"
        "v_exp_f32 v11, v11\n"
        "v_exp_f32 v12, v12\n"
        "v_exp_f32 v13, v13\n"
        "v_exp_f32 v14, v14\n"
        "v_exp_f32 v15, v15\n"
        "v_cvt_pk_bf16_f32 v0, v0, v1\n"
        "v_cvt_pk_bf16_f32 v1, v2, v3\n"
        "v_cvt_pk_bf16_f32 v2, v4, v5\n"
        "v_cvt_pk_bf16_f32 v3, v6, v7\n"
        "v_cvt_pk_bf16_f32 v8, v8, v9\n"
        "v_cvt_pk_bf16_f32 v9, v10, v11\n"
        "v_cvt_pk_bf16_f32 v10, v12, v13\n"
        "v_cvt_pk_bf16_f32 v11, v14, v15\n"
        "v_mfma_f32_16x16x32_bf16 v[32:35], v[84:87], v[0:3], 0\n"
        "v_mfma_f32_16x16x32_bf16 v[36:39], v[92:95], v[0:3], 0\n"
        "v_mfma_f32_16x16x32_bf16 v[32:35], v[88:91], v[8:11], v[32:35]\n"
        "v_mfma_f32_16x16x32_bf16 v[36:39], v[96:99], v[8:11], v[36:39]\n"
        "s_nop 7\n"
        "v_rcp_f32 v39, v38\n"
        "s_nop 0\n"
        "v_mul_f32 v32, v39, v32\n"
        "v_mul_f32 v33, v39, v33\n"
        "v_mul_f32 v34, v39, v34\n"
        "v_mul_f32 v35, v39, v35\n"
        "v_cvt_pk_bf16_f32 v32, v32, v33\n"
        "v_cvt_pk_bf16_f32 v33, v34, v35\n"
        "ds_write_b64 v81, v[32:33] offset:18944\n"
        "v_mul_f32 v36, v39, v36\n"
        "v_mul_f32 v37, v39, v37\n"
        "v_cvt_pk_bf16_f32 v36, v36, v37\n"
        "ds_write_b32 v82, v36 offset:18944\n"
        "v_mfma_f32_16x16x32_bf16 v[0:3], v[100:103], v[124:127], v[224:227]\n"
        "v_mfma_f32_16x16x32_bf16 v[4:7], v[104:107], v[124:127], v[228:231]\n"
        "v_mfma_f32_16x16x32_bf16 v[8:11], v[108:111], v[124:127], v[232:235]\n"
        "v_mfma_f32_16x16x32_bf16 v[12:15], v[112:115], v[124:127], v[236:239]\n"
        "s_waitcnt vmcnt(7)\n"
        "v_lshlrev_b32 v72, 16, v56\n"
        "v_and_b32 v73, 4294901760, v56\n"
        "v_lshlrev_b32 v74, 16, v57\n"
        "v_and_b32 v75, 4294901760, v57\n"
        "v_fmac_f32 v0, s80, v72\n"
        "v_fmac_f32 v1, s80, v73\n"
        "v_fmac_f32 v2, s80, v74\n"
        "v_fmac_f32 v3, s80, v75\n"
        "s_waitcnt vmcnt(6)\n"
        "v_lshlrev_b32 v72, 16, v58\n"
        "v_and_b32 v73, 4294901760, v58\n"
        "v_lshlrev_b32 v74, 16, v59\n"
        "v_and_b32 v75, 4294901760, v59\n"
        "v_fmac_f32 v4, s80, v72\n"
        "v_fmac_f32 v5, s80, v73\n"
        "v_fmac_f32 v6, s80, v74\n"
        "v_fmac_f32 v7, s80, v75\n"
        "s_waitcnt vmcnt(5)\n"
        "v_lshlrev_b32 v72, 16, v60\n"
        "v_and_b32 v73, 4294901760, v60\n"
        "v_lshlrev_b32 v74, 16, v61\n"
        "v_and_b32 v75, 4294901760, v61\n"
        "v_fmac_f32 v8, s80, v72\n"
        "v_fmac_f32 v9, s80, v73\n"
        "v_fmac_f32 v10, s80, v74\n"
        "v_fmac_f32 v11, s80, v75\n"
        "s_waitcnt vmcnt(4)\n"
        "v_lshlrev_b32 v72, 16, v62\n"
        "v_and_b32 v73, 4294901760, v62\n"
        "v_lshlrev_b32 v74, 16, v63\n"
        "v_and_b32 v75, 4294901760, v63\n"
        "v_fmac_f32 v12, s80, v72\n"
        "v_fmac_f32 v13, s80, v73\n"
        "v_fmac_f32 v14, s80, v74\n"
        "v_fmac_f32 v15, s80, v75\n"
        "v_max3_f32 v16, v0, v1, v2\n"
        "v_max3_f32 v16, v16, v3, v4\n"
        "v_max3_f32 v16, v16, v5, v6\n"
        "v_max3_f32 v16, v16, v7, v8\n"
        "v_max3_f32 v16, v16, v9, v10\n"
        "v_max3_f32 v16, v16, v11, v12\n"
        "v_max3_f32 v16, v16, v13, v14\n"
        "v_max_f32 v16, v16, v15\n"
        "v_mov_b32 v17, v16\n"
        "s_nop 1\n"
        "v_permlane16_swap_b32 v16, v17\n"
        "v_max_f32 v16, v16, v17\n"
        "v_mov_b32 v17, v16\n"
        "s_nop 1\n"
        "v_permlane32_swap_b32 v16, v17\n"
        "v_max_f32 v16, v16, v17\n"
        "v_sub_f32 v0, v0, v16\n"
        "v_sub_f32 v1, v1, v16\n"
        "v_sub_f32 v2, v2, v16\n"
        "v_sub_f32 v3, v3, v16\n"
        "v_sub_f32 v4, v4, v16\n"
        "v_sub_f32 v5, v5, v16\n"
        "v_sub_f32 v6, v6, v16\n"
        "v_sub_f32 v7, v7, v16\n"
        "v_sub_f32 v8, v8, v16\n"
        "v_sub_f32 v9, v9, v16\n"
        "v_sub_f32 v10, v10, v16\n"
        "v_sub_f32 v11, v11, v16\n"
        "v_sub_f32 v12, v12, v16\n"
        "v_sub_f32 v13, v13, v16\n"
        "v_sub_f32 v14, v14, v16\n"
        "v_sub_f32 v15, v15, v16\n"
        "v_exp_f32 v0, v0\n"
        "v_exp_f32 v1, v1\n"
        "v_exp_f32 v2, v2\n"
        "v_exp_f32 v3, v3\n"
        "v_exp_f32 v4, v4\n"
        "v_exp_f32 v5, v5\n"
        "v_exp_f32 v6, v6\n"
        "v_exp_f32 v7, v7\n"
        "v_exp_f32 v8, v8\n"
        "v_exp_f32 v9, v9\n"
        "v_exp_f32 v10, v10\n"
        "v_exp_f32 v11, v11\n"
        "v_exp_f32 v12, v12\n"
        "v_exp_f32 v13, v13\n"
        "v_exp_f32 v14, v14\n"
        "v_exp_f32 v15, v15\n"
        "v_cvt_pk_bf16_f32 v0, v0, v1\n"
        "v_cvt_pk_bf16_f32 v1, v2, v3\n"
        "v_cvt_pk_bf16_f32 v2, v4, v5\n"
        "v_cvt_pk_bf16_f32 v3, v6, v7\n"
        "v_cvt_pk_bf16_f32 v8, v8, v9\n"
        "v_cvt_pk_bf16_f32 v9, v10, v11\n"
        "v_cvt_pk_bf16_f32 v10, v12, v13\n"
        "v_cvt_pk_bf16_f32 v11, v14, v15\n"
        "v_mfma_f32_16x16x32_bf16 v[32:35], v[84:87], v[0:3], 0\n"
        "v_mfma_f32_16x16x32_bf16 v[36:39], v[92:95], v[0:3], 0\n"
        "v_mfma_f32_16x16x32_bf16 v[32:35], v[88:91], v[8:11], v[32:35]\n"
        "v_mfma_f32_16x16x32_bf16 v[36:39], v[96:99], v[8:11], v[36:39]\n"
        "s_nop 7\n"
        "v_rcp_f32 v39, v38\n"
        "s_nop 0\n"
        "v_mul_f32 v32, v39, v32\n"
        "v_mul_f32 v33, v39, v33\n"
        "v_mul_f32 v34, v39, v34\n"
        "v_mul_f32 v35, v39, v35\n"
        "v_cvt_pk_bf16_f32 v32, v32, v33\n"
        "v_cvt_pk_bf16_f32 v33, v34, v35\n"
        "ds_write_b64 v81, v[32:33] offset:19200\n"
        "v_mul_f32 v36, v39, v36\n"
        "v_mul_f32 v37, v39, v37\n"
        "v_cvt_pk_bf16_f32 v36, v36, v37\n"
        "ds_write_b32 v82, v36 offset:19200\n"
        "v_mfma_f32_16x16x32_bf16 v[0:3], v[100:103], v[128:131], v[240:243]\n"
        "v_mfma_f32_16x16x32_bf16 v[4:7], v[104:107], v[128:131], v[244:247]\n"
        "v_mfma_f32_16x16x32_bf16 v[8:11], v[108:111], v[128:131], v[248:251]\n"
        "v_mfma_f32_16x16x32_bf16 v[12:15], v[112:115], v[128:131], v[252:255]\n"
        "s_waitcnt vmcnt(3)\n"
        "v_lshlrev_b32 v72, 16, v64\n"
        "v_and_b32 v73, 4294901760, v64\n"
        "v_lshlrev_b32 v74, 16, v65\n"
        "v_and_b32 v75, 4294901760, v65\n"
        "v_fmac_f32 v0, s80, v72\n"
        "v_fmac_f32 v1, s80, v73\n"
        "v_fmac_f32 v2, s80, v74\n"
        "v_fmac_f32 v3, s80, v75\n"
        "s_waitcnt vmcnt(2)\n"
        "v_lshlrev_b32 v72, 16, v66\n"
        "v_and_b32 v73, 4294901760, v66\n"
        "v_lshlrev_b32 v74, 16, v67\n"
        "v_and_b32 v75, 4294901760, v67\n"
        "v_fmac_f32 v4, s80, v72\n"
        "v_fmac_f32 v5, s80, v73\n"
        "v_fmac_f32 v6, s80, v74\n"
        "v_fmac_f32 v7, s80, v75\n"
        "s_waitcnt vmcnt(1)\n"
        "v_lshlrev_b32 v72, 16, v68\n"
        "v_and_b32 v73, 4294901760, v68\n"
        "v_lshlrev_b32 v74, 16, v69\n"
        "v_and_b32 v75, 4294901760, v69\n"
        "v_fmac_f32 v8, s80, v72\n"
        "v_fmac_f32 v9, s80, v73\n"
        "v_fmac_f32 v10, s80, v74\n"
        "v_fmac_f32 v11, s80, v75\n"
        "s_waitcnt vmcnt(0)\n"
        "v_lshlrev_b32 v72, 16, v70\n"
        "v_and_b32 v73, 4294901760, v70\n"
        "v_lshlrev_b32 v74, 16, v71\n"
        "v_and_b32 v75, 4294901760, v71\n"
        "v_fmac_f32 v12, s80, v72\n"
        "v_fmac_f32 v13, s80, v73\n"
        "v_fmac_f32 v14, s80, v74\n"
        "v_fmac_f32 v15, s80, v75\n"
        "v_max3_f32 v16, v0, v1, v2\n"
        "v_max3_f32 v16, v16, v3, v4\n"
        "v_max3_f32 v16, v16, v5, v6\n"
        "v_max3_f32 v16, v16, v7, v8\n"
        "v_max3_f32 v16, v16, v9, v10\n"
        "v_max3_f32 v16, v16, v11, v12\n"
        "v_max3_f32 v16, v16, v13, v14\n"
        "v_max_f32 v16, v16, v15\n"
        "v_mov_b32 v17, v16\n"
        "s_nop 1\n"
        "v_permlane16_swap_b32 v16, v17\n"
        "v_max_f32 v16, v16, v17\n"
        "v_mov_b32 v17, v16\n"
        "s_nop 1\n"
        "v_permlane32_swap_b32 v16, v17\n"
        "v_max_f32 v16, v16, v17\n"
        "v_sub_f32 v0, v0, v16\n"
        "v_sub_f32 v1, v1, v16\n"
        "v_sub_f32 v2, v2, v16\n"
        "v_sub_f32 v3, v3, v16\n"
        "v_sub_f32 v4, v4, v16\n"
        "v_sub_f32 v5, v5, v16\n"
        "v_sub_f32 v6, v6, v16\n"
        "v_sub_f32 v7, v7, v16\n"
        "v_sub_f32 v8, v8, v16\n"
        "v_sub_f32 v9, v9, v16\n"
        "v_sub_f32 v10, v10, v16\n"
        "v_sub_f32 v11, v11, v16\n"
        "v_sub_f32 v12, v12, v16\n"
        "v_sub_f32 v13, v13, v16\n"
        "v_sub_f32 v14, v14, v16\n"
        "v_sub_f32 v15, v15, v16\n"
        "v_exp_f32 v0, v0\n"
        "v_exp_f32 v1, v1\n"
        "v_exp_f32 v2, v2\n"
        "v_exp_f32 v3, v3\n"
        "v_exp_f32 v4, v4\n"
        "v_exp_f32 v5, v5\n"
        "v_exp_f32 v6, v6\n"
        "v_exp_f32 v7, v7\n"
        "v_exp_f32 v8, v8\n"
        "v_exp_f32 v9, v9\n"
        "v_exp_f32 v10, v10\n"
        "v_exp_f32 v11, v11\n"
        "v_exp_f32 v12, v12\n"
        "v_exp_f32 v13, v13\n"
        "v_exp_f32 v14, v14\n"
        "v_exp_f32 v15, v15\n"
        "v_cvt_pk_bf16_f32 v0, v0, v1\n"
        "v_cvt_pk_bf16_f32 v1, v2, v3\n"
        "v_cvt_pk_bf16_f32 v2, v4, v5\n"
        "v_cvt_pk_bf16_f32 v3, v6, v7\n"
        "v_cvt_pk_bf16_f32 v8, v8, v9\n"
        "v_cvt_pk_bf16_f32 v9, v10, v11\n"
        "v_cvt_pk_bf16_f32 v10, v12, v13\n"
        "v_cvt_pk_bf16_f32 v11, v14, v15\n"
        "v_mfma_f32_16x16x32_bf16 v[32:35], v[84:87], v[0:3], 0\n"
        "v_mfma_f32_16x16x32_bf16 v[36:39], v[92:95], v[0:3], 0\n"
        "v_mfma_f32_16x16x32_bf16 v[32:35], v[88:91], v[8:11], v[32:35]\n"
        "v_mfma_f32_16x16x32_bf16 v[36:39], v[96:99], v[8:11], v[36:39]\n"
        "ds_read_b128 v[0:3], v103 offset:43264\n"
        "ds_read_b128 v[4:7], v103 offset:43328\n"
        "ds_read_b128 v[8:11], v103 offset:43392\n"
        "s_mov_b32 s79, 2139095040\n"
        "s_waitcnt vmcnt(0) lgkmcnt(0)\n"
        "s_nop 7\n"
        "s_cmp_lt_u32 s64, 8\n"
        "s_branch L_back%=\n"
        "L_sp3%=:\n"
        "s_mov_b32 s23, 0\n"
        "L_sp3l%=:\n"
        "s_sleep 1\n"
        "s_add_u32 s23, s23, 1\n"
        "s_cmp_gt_u32 s23, 131072\n"
        "s_cbranch_scc1 L_sp3b%=\n"
        "v_mov_b32 v58, s82\n"
        "ds_read_b64 v[58:59], v58\n"
        "s_waitcnt lgkmcnt(0)\n"
        "v_readfirstlane_b32 s77, v58\n"
        "v_readfirstlane_b32 s99, v59\n"
        "s_bitcmp1_b32 %[b], 8\n"
        "s_nop 2\n"
        "s_cselect_b32 s94, s99, s77\n"
        "s_cselect_b32 s99, s77, s99\n"
        "s_mov_b32 s77, s94\n"
        "s_and_b32 s94, s77, 2122219134\n"
        "s_cmp_lg_u32 s94, s98\n"
        "s_cbranch_scc1 L_sp3l%=\n"
        "s_branch L_sp3b%=\n"
        "L_sp17%=:\n"
        "s_mov_b32 s23, 0\n"
        "L_sp17l%=:\n"
        "s_sleep 1\n"
        "s_add_u32 s23, s23, 1\n"
        "s_cmp_gt_u32 s23, 131072\n"
        "s_cbranch_scc1 L_sp17b%=\n"
        "v_mov_b32 v74, s82\n"
        "ds_read_b64 v[74:75], v74\n"
        "s_waitcnt lgkmcnt(0)\n"
        "v_readfirstlane_b32 s77, v74\n"
        "v_readfirstlane_b32 s99, v75\n"
        "s_bitcmp1_b32 %[b], 8\n"
        "s_nop 2\n"
        "s_cselect_b32 s94, s99, s77\n"
        "s_cselect_b32 s99, s77, s99\n"
        "s_mov_b32 s77, s94\n"
        "s_and_b32 s94, s77, 2122219134\n"
        "s_cmp_lg_u32 s94, s98\n"
        "s_cbranch_scc1 L_sp17l%=\n"
        "s_branch L_sp17b%=\n"
        "L_sp20%=:\n"
        "s_mov_b32 s23, 0\n"
        "L_sp20l%=:\n"
        "s_sleep 1\n"
        "s_add_u32 s23, s23, 1\n"
        "s_cmp_gt_u32 s23, 131072\n"
        "s_cbranch_scc1 L_sp20b%=\n"
        "v_mov_b32 v54, s82\n"
        "ds_read_b64 v[54:55], v54\n"
        "s_waitcnt lgkmcnt(0)\n"
        "v_readfirstlane_b32 s77, v54\n"
        "v_readfirstlane_b32 s99, v55\n"
        "s_bitcmp1_b32 %[b], 8\n"
        "s_nop 2\n"
        "s_cselect_b32 s94, s99, s77\n"
        "s_cselect_b32 s99, s77, s99\n"
        "s_mov_b32 s77, s94\n"
        "s_and_b32 s94, s77, 2122219134\n"
        "s_cmp_lg_u32 s94, s98\n"
        "s_cbranch_scc1 L_sp20l%=\n"
        "s_branch L_sp20b%=\n"
        "L_sp21%=:\n"
        "s_mov_b32 s23, 0\n"
        "L_sp21l%=:\n"
        "s_sleep 1\n"
        "s_add_u32 s23, s23, 1\n"
        "s_cmp_gt_u32 s23, 131072\n"
        "s_cbranch_scc1 L_sp21b%=\n"
        "v_mov_b32 v77, s50\n"
        "ds_read_b32 v77, v77\n"
        "s_waitcnt lgkmcnt(0)\n"
        "v_readfirstlane_b32 s95, v77\n"
        "s_nop 3\n"
        "s_and_b32 s94, s95, 2122219134\n"
        "s_cmp_lg_u32 s94, s48\n"
        "s_cbranch_scc1 L_sp21l%=\n"
        "s_branch L_sp21b%=\n"
        "L_sp26%=:\n"
        "s_mov_b32 s23, 0\n"
        "L_sp26l%=:\n"
        "s_sleep 1\n"
        "s_add_u32 s23, s23, 1\n"
        "s_cmp_gt_u32 s23, 131072\n"
        "s_cbranch_scc1 L_sp26b%=\n"
        "v_mov_b32 v74, s82\n"
        "ds_read_b64 v[74:75], v74\n"
        "s_waitcnt lgkmcnt(0)\n"
        "v_readfirstlane_b32 s77, v74\n"
        "v_readfirstlane_b32 s99, v75\n"
        "s_bitcmp1_b32 %[b], 8\n"
        "s_nop 2\n"
        "s_cselect_b32 s94, s99, s77\n"
        "s_cselect_b32 s99, s77, s99\n"
        "s_mov_b32 s77, s94\n"
        "s_and_b32 s94, s77, 2122219134\n"
        "s_cmp_lg_u32 s94, s98\n"
        "s_cbranch_scc1 L_sp26l%=\n"
        "s_branch L_sp26b%=\n"
        "L_end%=:\n"
        :
        : [x] "s"(x), [w] "s"(wqkv), [wo] "s"(wo), [r] "s"(rpb), [i] "s"(idx), [m] "s"(mask), [o] "s"(out), [b] "s"(bid), [wid] "s"(wid), [lb] "s"(lbase)
        : "v0", "v1", "v2", "v3", "v4", "v5", "v6", "v7", "v8", "v9", "v10", "v11", "v12", "v13", "v14", "v15", "v16", "v17", "v18", "v19", "v20", "v21", "v22", "v23", "v24", "v25", "v26", "v27", "v28", "v29", "v30", "v31", "v32", "v33", "v34", "v35", "v36", "v37", "v38", "v39", "v40", "v41", "v42", "v43", "v44", "v45", "v46", "v47", "v48", "v49", "v50", "v51", "v52", "v53", "v54", "v55", "v56", "v57", "v58", "v59", "v60", "v61", "v62", "v63", "v64", "v65", "v66", "v67", "v68", "v69", "v70", "v71", "v72", "v73", "v74", "v75", "v76", "v77", "v78", "v79", "v80", "v81", "v82", "v83", "v84", "v85", "v86", "v87", "v88", "v89", "v90", "v91", "v92", "v93", "v94", "v95", "v96", "v97", "v98", "v99", "v100", "v101", "v102", "v103", "v104", "v105", "v106", "v107", "v108", "v109", "v110", "v111", "v112", "v113", "v114", "v115", "v116", "v117", "v118", "v119", "v120", "v121", "v122", "v123", "v124", "v125", "v126", "v127", "v128", "v129", "v130", "v131", "v132", "v133", "v134", "v135", "v136", "v137", "v138", "v139", "v140", "v141", "v142", "v143", "v144", "v145", "v146", "v147", "v148", "v149", "v150", "v151", "v152", "v153", "v154", "v155", "v156", "v157", "v158", "v159", "v160", "v161", "v162", "v163", "v164", "v165", "v166", "v167", "v168", "v169", "v170", "v171", "v172", "v173", "v174", "v175", "v176", "v177", "v178", "v179", "v180", "v181", "v182", "v183", "v184", "v185", "v186", "v187", "v188", "v189", "v190", "v191", "v192", "v193", "v194", "v195", "v196", "v197", "v198", "v199", "v200", "v201", "v202", "v203", "v204", "v205", "v206", "v207", "v208", "v209", "v210", "v211", "v212", "v213", "v214", "v215", "v216", "v217", "v218", "v219", "v220", "v221", "v222", "v223", "v224", "v225", "v226", "v227", "v228", "v229", "v230", "v231", "v232", "v233", "v234", "v235", "v236", "v237", "v238", "v239", "v240", "v241", "v242", "v243", "v244", "v245", "v246", "v247", "v248", "v249", "v250", "v251", "v252", "v253", "v254", "v255", "s20", "s21", "s22", "s23", "s24", "s25", "s26", "s27", "s28", "s29", "s30", "s31", "s33", "s34", "s35", "s36", "s37", "s38", "s39", "s40", "s41", "s42", "s43", "s44", "s45", "s46", "s47", "s48", "s49", "s50", "s51", "s52", "s53", "s54", "s55", "s56", "s57", "s58", "s59", "s60", "s61", "s62", "s63", "s64", "s65", "s66", "s67", "s68", "s69", "s70", "s71", "s72", "s73", "s74", "s75", "s76", "s77", "s78", "s79", "s80", "s81", "s82", "s83", "s84", "s85", "s86", "s87", "s88", "s89", "s90", "s91", "s92", "s93", "s94", "s95", "s96", "s97", "s98", "s99", "vcc", "scc", "memory");
}

extern "C" void kernel_launch(void* const* d_in, const int* in_sizes, int n_in, void* d_out, int out_size, void* d_ws, size_t ws_size, hipStream_t stream) {
    swin_window_attn_kernel<<<256, 512, 0, stream>>>(d_in[0], d_in[1], d_in[2], d_in[3], d_in[4], d_in[5], d_out);
}
```
